```python
import torch

def _softmax(x, axis=-1):
    return torch.softmax(x, dim=axis)

def _gelu(x):
    return torch.nn.functional.gelu(x, approximate='tanh')

def _silu(x):
    return torch.nn.functional.silu(x)

def _sigmoid(x):
    return torch.sigmoid(x)

def _layernorm(x, axis=-1, eps=1e-05):
    m = x.mean(dim=axis, keepdim=True)
    v = x.var(dim=axis, keepdim=True, unbiased=False)
    return (x - m) / torch.sqrt(v + eps)

def _scatter_add(tgt, idx, src, axis=0):
    return tgt.clone().index_add_(axis, idx, src)

def _segment_sum(src, seg, n):
    out = torch.zeros((n,) + src.shape[1:], dtype=src.dtype)
    return out.index_add_(0, seg, src)

def _pad(x, pw):
    flat = tuple((v for pair in reversed(pw) for v in pair))
    return torch.nn.functional.pad(x, flat)
kernel_name = 'gated_mix__memory_aug_recurrent_mem_tr__spectral_gfnet__prompt'

def setup_inputs():

    _w = lambda a: (lambda t: t.to(torch.bfloat16) if t.is_floating_point() else t)(torch.as_tensor(a))

    def _A_setup():
        B, S, Dm = (1, 2048, 1024)
        H, Dh = (32, 128)
        M, seg = (8, 32)
        seg = min(seg, S)
        S = S // seg * seg
        return {'x': _w((lambda *s: torch.randn(*s, dtype=torch.bfloat16) * 0.05)(B, S, Dm)), 'mem0': _w((lambda *s: torch.randn(*s, dtype=torch.bfloat16) * 0.4)(B, M, Dm)), 'Wq': _w((lambda *s: torch.randn(*s, dtype=torch.bfloat16) * 6.4)(Dm, H, Dh) * Dm ** (-0.5)), 'Wk': _w((lambda *s: torch.randn(*s, dtype=torch.bfloat16) * 6.4)(Dm, H, Dh) * Dm ** (-0.5)), 'Wv': _w((lambda *s: torch.randn(*s, dtype=torch.bfloat16) * 0.4)(Dm, H, Dh) * Dm ** (-0.5)), 'Wo': _w((lambda *s: torch.randn(*s, dtype=torch.bfloat16) * 0.4)(H, Dh, Dm) * (H * Dh) ** (-0.5)), 'seg_len': seg}
    _A_in = _A_setup()

    def _B_setup():
        B, S, D = (1, 2048, 1024)
        Df = D // 2 + 1
        return {'x': _w((lambda *s: torch.randn(*s, dtype=torch.bfloat16) * 0.05)(B, S, D)), 'w_r': _w((lambda *s: torch.randn(*s, dtype=torch.bfloat16) * 3.2)(S, Df) * 0.02), 'w_i': _w((lambda *s: torch.randn(*s, dtype=torch.bfloat16) * 3.2)(S, Df) * 0.02)}
    _B_in = _B_setup()
    return {'A_x': _A_in['x'], 'A_mem0': _A_in['mem0'], 'A_Wq': _A_in['Wq'], 'A_Wk': _A_in['Wk'], 'A_Wv': _A_in['Wv'], 'A_Wo': _A_in['Wo'], 'A_seg_len': _A_in['seg_len'], 'B_x': _B_in['x'], 'B_w_r': _B_in['w_r'], 'B_w_i': _B_in['w_i'], 'gate_w': _w((lambda *s: torch.randn(*s, dtype=torch.bfloat16) * 0.05)(1024) * 16.0)}

def reference(A_x, A_mem0, A_Wq, A_Wk, A_Wv, A_Wo, A_seg_len, B_x, B_w_r, B_w_i, gate_w):

    def _A_ref(x, mem0, Wq, Wk, Wv, Wo, seg_len):
        B, S, Dm = x.shape
        H, Dh = (Wq.shape[1], Wq.shape[2])
        M = mem0.shape[1]
        scale = Dh ** (-0.5)
        n_seg = S // seg_len
        L = M + seg_len
        idx = torch.arange(L)
        mask = (idx[:, None] < M) | (idx[:, None] >= idx[None, :])
        mem = mem0
        outs = []
        for s in range(n_seg):
            xs = x[:, s * seg_len:(s + 1) * seg_len, :].clone()
            h = torch.concatenate([mem, xs], axis=1)
            q = torch.einsum('btd,dhe->bhte', h, Wq)
            k = torch.einsum('btd,dhe->bhte', h, Wk)
            v = torch.einsum('btd,dhe->bhte', h, Wv)
            scores = torch.einsum('bhqe,bhke->bhqk', q, k) * scale
            scores = torch.where(mask[None, None], scores, -1000000000.0)
            p = _softmax(scores, axis=-1)
            o = torch.einsum('bhqk,bhke->bhqe', p, v)
            o = torch.einsum('bhte,hed->btd', o, Wo)
            mem = o[:, :M, :].clone()
            outs.append(o[:, M:, :])
        return torch.concatenate(outs, axis=1)

    def _B_ref(x, w_r, w_i):
        B, S, D = x.shape
        X = (lambda x, axes=(-2, -1): torch.fft.rfft2(x.float(), dim=axes))(x, axes=(-2, -1))
        W = w_r + 1j * w_i
        X = X * W
        y = (lambda x, s, axes=(-2, -1): torch.fft.irfft2(x, s=s, dim=axes).to(torch.bfloat16))(X, (S, D), axes=(-2, -1))
        return y
    x = A_x
    g = _sigmoid((x * gate_w).sum(-1, keepdims=True))
    a = _A_ref(x, A_mem0, A_Wq, A_Wk, A_Wv, A_Wo, A_seg_len)
    b = _B_ref(x, B_w_r, B_w_i)
    return g * a + (1 - g) * b
```

```cpp
#include <hip/hip_runtime.h>
#include <hip/hip_bf16.h>
#include <stdint.h>
#include <type_traits>

typedef short bf16x8 __attribute__((ext_vector_type(8)));
typedef short s16x4 __attribute__((ext_vector_type(4)));
typedef float f32x4 __attribute__((ext_vector_type(4)));
typedef unsigned u32x4 __attribute__((ext_vector_type(4)));
typedef unsigned u32x2 __attribute__((ext_vector_type(2)));
typedef unsigned short u16;
typedef __amdgpu_buffer_rsrc_t rsrc_t;
typedef __attribute__((address_space(3))) char* ldsp;
typedef __attribute__((address_space(3))) u32x4* lp4;
typedef __attribute__((address_space(3))) u32x2* lp2;
typedef __attribute__((address_space(3))) unsigned* lp1;

#define DEVI __device__ __forceinline__

DEVI float bf2f(u16 v) { return __uint_as_float(((unsigned)v) << 16); }
DEVI u16 f2bf(float f) { return __builtin_bit_cast(u16, (__bf16)f); }
DEVI float rbf(float f) { return bf2f(f2bf(f)); }
DEVI unsigned pack2(u16 a, u16 b) { return (unsigned)a | ((unsigned)b << 16); }
DEVI f32x4 mfma16(bf16x8 a, bf16x8 b, f32x4 c) { return __builtin_amdgcn_mfma_f32_16x16x32_bf16(a, b, c, 0, 0, 0); }
DEVI rsrc_t mkrsrc(const void* p, unsigned bytes) { return __builtin_amdgcn_make_buffer_rsrc((void*)p, 0, bytes, 0x00020000); }
DEVI u32x4 ld_sc1(rsrc_t r, unsigned off) { return __builtin_amdgcn_raw_buffer_load_b128(r, off, 0, 16); }
DEVI unsigned ld_sc1_32(rsrc_t r, unsigned off) { return __builtin_amdgcn_raw_buffer_load_b32(r, off, 0, 16); }
DEVI void st_sc1(rsrc_t r, unsigned off, u32x4 v) { __builtin_amdgcn_raw_buffer_store_b128(v, r, off, 0, 16); }
DEVI void st_sc1_64(rsrc_t r, unsigned off, u32x2 v) { __builtin_amdgcn_raw_buffer_store_b64(v, r, off, 0, 16); }
DEVI void st_sc1_32(rsrc_t r, unsigned off, unsigned v) { __builtin_amdgcn_raw_buffer_store_b32(v, r, off, 0, 16); }
DEVI int tid() { int t = threadIdx.x; asm volatile("" : "+v"(t)); return t; }
#define FNT 1
template <bool NT, class T> DEVI T ldg(const T* p) { return NT ? __builtin_nontemporal_load(p) : *p; }
DEVI void drain_vm() { asm volatile("s_waitcnt vmcnt(0)" ::: "memory"); }

constexpr int S_ = 2048, D_ = 1024, H_ = 32, DH_ = 128, MEM_ = 8, SEG_ = 32, NSEG_ = 64, LK_ = 40;
constexpr int HD_ = H_ * DH_;
constexpr int FB_ = 513;

constexpr unsigned C_KV = 0;
constexpr unsigned C_MEM = C_KV + 512;
constexpr unsigned C_Q = C_MEM + 2048;
constexpr unsigned C_O = C_Q + 64;
constexpr unsigned C_P = C_O + 256;
constexpr unsigned C_FFT = C_P + 96;
constexpr unsigned C_TICKET = C_FFT + 4;
constexpr unsigned C_ABORT = C_TICKET + 32;
constexpr unsigned C_SLOTS = C_ABORT + 1;
constexpr int WAIT_CAP = 1 << 19;

constexpr unsigned CH_R1 = 1, CH_R2 = CH_R1 + 8u, CH_R3 = CH_R2 + 32u, CH_RD = CH_R3 + 64u, NCH = CH_RD + 1u;
constexpr unsigned NPG = 117;
#define V13
constexpr unsigned W_MB = (256u + 112u) << 14, W_PG = W_MB + 1024u, W_TW = W_MB + 2048u, W_MSG = W_MB + 8192u, W_HI = W_MB + 16384u, W_RL = W_MB + 32768u, W_ST = W_MB + 65536u;
#define STS 32u
constexpr int P_CNT = 3, P_Q = 4, P_K = 20, P_VX = 36, P_VM = 52, P_KM = 56, P_B = 60, P_O = 64, P_T1 = 80, P_T2 = 88, P_PT = 96, P_FT = 112;
constexpr unsigned W_END = NPG << 21;
static_assert(NCH <= 112u && NCH + 8u <= 124u && W_ST + 2048u * STS <= (380u << 14) && C_SLOTS <= 64u * 128u, "workspace map");
struct Far {
  unsigned long long m0, m1;
  template <int P> DEVI unsigned blk(unsigned b) const {
    return ((unsigned)P << 21) + (b << 14) + (((unsigned)((P < 64 ? m0 : m1) >> ((P & 63) + (b >> 7))) & 1u) << 13);
  }
};
#define GATE(fm) (fm.blk<P_CNT>(64) ^ 8192u)

#define CSLP 16
#define CSLF 8
#define CSLL 16
#define CFAR C_Q
DEVI unsigned* cix(unsigned* cnt, unsigned slot) {
  return cnt + (int)((slot + (slot & ~127u)) * 16u + (slot < CFAR ? 0u : 2048u - (((unsigned)(size_t)cnt >> 1) & 4096u)));
}
DEVI void cnt_add(unsigned* cnt, unsigned slot, unsigned n = 1u) { __hip_atomic_fetch_add(cix(cnt, slot), n, __ATOMIC_RELAXED, __HIP_MEMORY_SCOPE_AGENT); }
template <int SL = CSLP>
DEVI void cnt_wait(unsigned* cnt, unsigned slot, unsigned n) {
  for (int it = 0; it < WAIT_CAP; ++it) {
    if (__hip_atomic_load(cix(cnt, slot), __ATOMIC_RELAXED, __HIP_MEMORY_SCOPE_AGENT) >= n) return;
    if ((it & 63) == 63 && __hip_atomic_load(cix(cnt, C_ABORT), __ATOMIC_RELAXED, __HIP_MEMORY_SCOPE_AGENT) != 0u) return;
    __builtin_amdgcn_s_sleep(SL > 127 ? 127 : SL);
    if (SL > 127) __builtin_amdgcn_s_sleep(SL > 254 ? 127 : SL - 127);
  }
  __hip_atomic_store(cix(cnt, C_ABORT), 1u, __ATOMIC_RELAXED, __HIP_MEMORY_SCOPE_AGENT);
}

constexpr int MST = 192;
struct Tag { u32x2 t; int vid, vote; };
DEVI u32x2 ld_sc1_64(rsrc_t r, unsigned off) { return __builtin_amdgcn_raw_buffer_load_b64(r, off, 0, 16); }
DEVI bool same(u32x2 a, u32x2 b) { return a[0] == b[0] && a[1] == b[1]; }
DEVI unsigned rl_off(int vid) { return W_RL + (unsigned)vid * 128u + (threadIdx.x & 1u) * 16u; }
DEVI void report(rsrc_t rw, const Tag& g) {
  u32x2 a = ld_sc1_64(rw, W_HI + MST * 32u);
  drain_vm();
  if (g.vid == MST) a = g.t;
  st_sc1(rw, (threadIdx.x & 63u) ? ~0u : W_MSG + (unsigned)(g.vid - 192) * 128u + (threadIdx.x >> 2), u32x4{a[0], a[1], (unsigned)g.vote, 0u});
}
DEVI Far accept(rsrc_t rw, const Tag& g, u32x4 r) {
#pragma nounroll
  for (int it = 0; it < WAIT_CAP && !__all(r[0] == g.t[0] && r[1] == g.t[1]); ++it) {
    asm volatile("s_sleep 2" ::: "memory");
    if (g.vid >= 192) report(rw, g);
    r = ld_sc1(rw, rl_off(g.vid));
    asm volatile("" : "+v"(r));
  }
  u32x2 ack = ~g.t;
  asm volatile("" : "+s"(ack));
  st_sc1_64(rw, threadIdx.x == 0 ? W_HI + (unsigned)g.vid * 32u : ~0u, ack);
  asm volatile("" ::: "memory");
  const Far fm = {(unsigned long long)__builtin_amdgcn_readlane(r[3], 0) << 32 | (unsigned)__builtin_amdgcn_readlane(r[2], 0),
                  (unsigned long long)__builtin_amdgcn_readlane(r[3], 1) << 32 | (unsigned)__builtin_amdgcn_readlane(r[2], 1)};
  if (g.vid == MST) {
    const unsigned t = threadIdx.x, o = W_RL + (t >> 1) * 128u + (t & 1u) * 16u;
#pragma nounroll
    for (int it = 0; it < WAIT_CAP; ++it) {
      const u32x2 a = ld_sc1_64(rw, W_HI + (t >> 1) * 32u), b = ld_sc1_64(rw, o);
      if (__all(same(a, ~b))) break;
      st_sc1(rw, same(a, ~b) || same(a, b) ? ~0u : o, u32x4{a[0], a[1], r[2], r[3]});
      asm volatile("s_waitcnt vmcnt(0)\n s_sleep 8" ::: "memory");
    }
  }
  return fm;
}
#define PSS 56
constexpr int PS0 = PSS, PSO = PSS;
#define NOS 20
constexpr int NOL = (2 * NSEG_ - 6 * NOS + 2) / 3;
static_assert(3 * NOS >= PS0 && NOS % 2 == 0, "last segments");
DEVI unsigned clk32() { unsigned long long t; asm volatile("s_memtime %0\n s_waitcnt lgkmcnt(0)" : "=s"(t) :: "memory"); return (unsigned)t; }
DEVI unsigned max4(unsigned m, u32x4 v) { return max(max(m, max(v[0], v[1])), max(v[2], v[3])); }

DEVI unsigned cvt2(float a, float b) {
  typedef __bf16 bf2 __attribute__((ext_vector_type(2)));
  typedef float f2 __attribute__((ext_vector_type(2)));
  return __builtin_bit_cast(unsigned, __builtin_convertvector(f2{a, b}, bf2));
}
#define OCT(g) ((((g) & 1) << 1) | ((g) >> 1))
DEVI u32x4 octet(f32x4 a, f32x4 b) {
  const auto s0 = __builtin_amdgcn_permlane16_swap(cvt2(a[0], a[1]), cvt2(b[0], b[1]), false, false);
  const auto s1 = __builtin_amdgcn_permlane16_swap(cvt2(a[2], a[3]), cvt2(b[2], b[3]), false, false);
  return u32x4{s0[0], s1[0], s0[1], s1[1]};
}
DEVI float fmx(float a, float b) { float r; asm("v_max_f32 %0, %1, %2" : "=v"(r) : "v"(a), "v"(b)); return r; }
DEVI float fmx3(float a, float b, float c) { float r; asm("v_max3_f32 %0, %1, %2, %3" : "=v"(r) : "v"(a), "v"(b), "v"(c)); return r; }
DEVI float rows_max(float x) {
  const unsigned u = __float_as_uint(x);
  auto a = __builtin_amdgcn_permlane16_swap(u, u, false, false);
  x = fmx(__uint_as_float(a[0]), __uint_as_float(a[1]));
  const unsigned u2 = __float_as_uint(x);
  auto b = __builtin_amdgcn_permlane32_swap(u2, u2, false, false);
  return fmx(__uint_as_float(b[0]), __uint_as_float(b[1]));
}
DEVI float rows_sum(float x) {
  const unsigned u = __float_as_uint(x);
  auto a = __builtin_amdgcn_permlane16_swap(u, u, false, false);
  x = __uint_as_float(a[0]) + __uint_as_float(a[1]);
  const unsigned u2 = __float_as_uint(x);
  auto b = __builtin_amdgcn_permlane32_swap(u2, u2, false, false);
  return __uint_as_float(b[0]) + __uint_as_float(b[1]);
}


constexpr int GEMM_LDS = 131072;
#define SNT "nt"
#define XR 2
#define PFD 4
#define SIG0 24
#define Q_WV(n) asm volatile("s_waitcnt vmcnt(" #n ")" ::: "memory")
#define Q_WL(n) asm volatile("s_waitcnt lgkmcnt(" #n ")" ::: "memory")
#define Q_BAR __builtin_amdgcn_s_barrier()
#define Q_SCHED __builtin_amdgcn_sched_barrier(0)

#define PBU 4
#define PM 10
constexpr int pro(int r) { return r < XR ? 0 : (2 * r + 2 < PBU * (r - XR + 1) ? 2 * r + 2 : PBU * (r - XR + 1)); }
static_assert(pro(7) == 16, "all k-steps");
#define QSA "................0h2"
#define QSB "f0geFs...........2....."
constexpr int qpos(const char* q, char c) { int i = 0; while (q[i] && q[i] != c) ++i; return i; }
constexpr int qcnt(const char* q, int n, char c) { int k = 0; for (int i = 0; i < n && q[i]; ++i) k += q[i] == c; return k; }
static_assert(qcnt(QSA, 99, '.') == 16 && qcnt(QSB, 99, '.') == 16, "stage");
#define PSLO 4
#define KVX 8192u
template <bool isV, int kh, bool isO = false>
DEVI Far qkv_server(char* lds, const int mat, const int hh, const u16* __restrict__ x, const u16* __restrict__ W, char* ws, rsrc_t rws, const Tag& tag, Far fm = {0ull, 0ull}, const u16* wop = nullptr) {
  typedef const __attribute__((address_space(3))) bf16x8* lpf;
  typedef __attribute__((address_space(3))) f32x4* lpa;
  constexpr unsigned WR = isO ? D_ * 2u : HD_ * 2u;
  constexpr int NS = isO ? NOS : NSEG_, FST = isO ? 8192 : 256;
  const int t = tid(), l = t & 63, w = __builtin_amdgcn_readfirstlane(t >> 6), wc = w & 3, fq = l >> 4, fr = l & 15;
  const ldsp lb = (ldsp)lds;
  const unsigned lbu = (unsigned)(size_t)lb;
  const rsrc_t rx = mkrsrc(x, S_ * D_ * 2u), rc = mkrsrc(W, D_ * HD_ * 2u);
  constexpr int RING = 32768;
  unsigned voffA[4];
#pragma unroll
  for (int p = 0; p < 4; ++p) voffA[p] = isO ? (unsigned)(((wc >> 1) + 2 * (kh ^ 1)) * 16384 + (wc & 1) * 4096 + (p ^ 1) * 1024 + l * 16)
                                                 : (unsigned)(wc * (isV ? 16384 : 8192) + (p & 2) * 2048 + (kh ^ 1) * 1024 + (l ^ ((p & 1) * 8 + 2 * wc + (p >> 1))) * 16);
  unsigned* cnt = (unsigned*)(ws + fm.blk<P_CNT>(0));
  unsigned rdy = ~0u << NOS;
  auto need = [&](int n) {
#pragma nounroll
    for (int it = 0; it < WAIT_CAP && !((rdy >> n) & 1u); ++it) {
      if (it) __builtin_amdgcn_s_sleep(CSLF);
      rdy |= (unsigned)__builtin_amdgcn_ballot_w64(__hip_atomic_load(cix(cnt, C_O + (3 * min(tid() & 63, NOS - 1) + mat) * 4 + (hh >> 3)), __ATOMIC_RELAXED, __HIP_MEMORY_SCOPE_AGENT) >= 8u);
    }
    asm volatile("" ::: "memory");
  };
  const unsigned ldsA = lbu + (unsigned)(RING + wc * 8192 + (kh ^ 1) * 1024);
  auto pair = [&](int s, int d, int p) {
    unsigned so;
    if (isO) {
      const int n = s + (d >> 1);
      so = n < NOS ? fm.blk<P_O>((unsigned)((6 * n + 2 * mat + (d & 1)) * 16 + (hh >> 3) * 4)) - 1024u : 1u << 31;
      asm volatile("s_add_u32 m0, %4, %5\n s_nop 0\n buffer_load_dwordx4 %0, %2, %3 offen sc1 lds\n buffer_load_dwordx4 %1, %2, %3 offen offset:2048 sc1 lds"
                   :: "v"(voffA[p]), "v"(voffA[p + 1]), "s"(rws), "s"(so), "s"(ldsA), "i"((d & 3) * 32768 + p * 2048) : "memory", "scc");
    } else
    asm volatile("s_add_u32 m0, %5, %6\n s_add_u32 %0, %4, %7\n buffer_load_dwordx4 %1, %3, %0 offen lds\n buffer_load_dwordx4 %2, %3, %0 offen offset:2048 lds"
                 : "=&s"(so) : "v"(voffA[p]), "v"(voffA[p + 1]), "s"(rx), "s"((unsigned)s * 65536u), "s"(ldsA), "i"((d & 3) * 32768 + p * 2048),
                   "i"((d >> 1) * 65536 + (d & 1) * (isV ? 8192 : 32768)) : "memory", "scc");
  };
  auto stage = [&](int n) { pair(0, n, 0); pair(0, n, 2); };
  const unsigned voffP = mat && !isO ? (unsigned)((hh & 7) * 8192 + t * 16) : 1u << 31;
  bf16x8 wr[16][2];
  ldsp pa[8];
#pragma unroll
  for (int i = 0; i < 8; ++i) {
    pa[i] = lb + RING + (i >> 2) * 65536 + kh * 1024 + (isO ? (i & 3) * 2048 + l * 16 : fr * 2048 + ((4 * (i & 3) + fq) ^ ((fr & 1) << 3) ^ (fr >> 1)) * 16);
    asm volatile("" : "+v"(pa[i]));
  }
  const unsigned on = (unsigned)(isV ? wc * 2048 + l * 16 : wc * 1024 + (mat && !isO ? (fr & 2) * 2048 + fq * 256 + (2 * (fr >> 2) + (fr & 1)) * 16 : l * 16));
  const unsigned od = isV ? 1024u : (mat && !isO ? 128u : 4096u);
  unsigned* prg = cix(cnt, C_P + mat * 32 + hh);
  constexpr int PD = 6;
  bf16x8 af[8];
  auto frag = [&](int i) { af[i & 7] = *(lpf)(pa[(i & 3) + ((i >> 3) & 4)] + ((i >> 4) & 1) * 32768 + ((i >> 2) & 3) * FST); };
  f32x4 acc[4][2];
#pragma unroll
  for (int i = 0; i < 8; ++i) acc[i >> 1][i & 1] = f32x4{0.f, 0.f, 0.f, 0.f};
  auto put = [&](int s, unsigned o, u32x4 v) {
    __builtin_amdgcn_raw_buffer_store_b128(v, rws, on, (isO ? fm.blk<P_PT>((unsigned)((3 * s + mat) * H_ + hh)) : fm.blk<P_Q>((unsigned)(mat * 2048 + s * H_ + hh)) ^ (mat ? KVX : 0u)) + o, 16);
  };
  auto lastp = [&](int g) { return g < 2 * NSEG_ ? fm.blk<P_PT>((unsigned)((g >> 1) * H_ + hh)) + (unsigned)(g & 1) * 4096u : 1u << 31; };
  if (isO && kh) {
#pragma unroll
    for (int i = 0; i < NOL; ++i) st_sc1(rws, on + lastp(6 * NOS + 3 * i + mat), u32x4{~0u, ~0u, ~0u, ~0u});
  }
  {
    const unsigned voffB = (unsigned)(4 * w + fq) * WR + (unsigned)((fr ^ (2 * (fq | ((w & 2) << 1)))) * 16);
    u32x4 du[PFD], rel;
#define WBUF(r) ((r) % 3 == 1 ? 0 : RING + 65536 + (r) % 3 * 16384)
    auto round = [&](int r) {
      unsigned so;
#pragma unroll
      for (int i = 0; i < 4; ++i)
        if (isO) asm volatile("s_add_u32 m0, %4, %5\n s_add_u32 %0, %3, %6\n s_nop 2\n buffer_load_dwordx4 %1, %2, %0 offen lds" : "=&s"(so) : "v"(voffB), "s"(rc), "s"((unsigned)(hh >> 3) * 1024u * WR + (unsigned)(hh & 7) * 256u),
                     "s"(lbu + (unsigned)w * 1024u), "i"(WBUF(r) + i * 8192), "i"((((i & 2) * 4 + r) * 64 + 32 * (i & 1)) * WR) : "memory", "scc");
        else asm volatile("s_add_u32 m0, %4, %5\n s_add_u32 %0, %3, %6\n s_nop 2\n buffer_load_dwordx4 %1, %2, %0 offen " SNT " lds" : "=&s"(so) : "v"(voffB), "s"(rc), "s"((unsigned)hh * 256u),
                     "s"(lbu + (unsigned)w * 1024u), "i"(WBUF(r) + i * 8192), "i"((((i & 2) * 4 + r) * 64 + 32 * (i & 1)) * WR) : "memory", "scc");
    };
    auto ahead = [&](int n, int s) { asm volatile("s_nop 4\n buffer_load_dwordx4 %0, %1, %2, %3 offen" : "=v"(du[n]) : "v"(voffP), "s"(rx), "s"(s * 65536)); };
    if (isO) { need(0); need(1); need(2); need(3); }
    ahead(0, mat - 1);
#pragma unroll
    for (int r = 0; r < 2; ++r) { round(r); Q_BAR; }
    ldsp pb[2];
#pragma unroll
    for (int j = 0; j < 2; ++j) pb[j] = lb + kh * 16384 + (8 * fq + (fr >> 2)) * 256 + (((2 * wc + ((fr >> 1) & 1)) ^ ((fr >> 2) | ((fq & 1) << 2))) * 32) + 16 * (fr & 1) + 8 * j;
    auto fetchx = [&](int n) {
#pragma unroll
      for (int d = 0; d < 2; ++d) af[2 * (n & 3) + d] = *(lpf)(pa[(n & 3) + 4 * (n >> 4)] + d * 32768 + ((n >> 2) & 3) * FST);
    };
    auto kstep = [&](int n) {
#pragma unroll
      for (int i = 0; i < 4; ++i) {
        f32x4& c = acc[2 * (n >> 4) + (i >> 1)][i & 1];
        const bf16x8 a = af[2 * (n & 3) + (i >> 1)], b = wr[n & 15][i & 1];
        c = isV ? mfma16(a, b, c) : mfma16(b, a, c);
      }
      Q_SCHED;
      if ((n & 15) < 12) fetchx(n + 4);
      Q_SCHED;
    };
    auto first = [&](int s) {
#pragma unroll
      for (int i = 0; i < 2; ++i) *(lpa)(lb + (2 * s + i) * 8192 + t * 16) = isV ? acc[2 * s + i][kh ^ 1] : acc[2 * s + (kh ^ 1)][i];
      if (s == 0 && !isO) {
        asm volatile("" : "+v"(rel));
        fm = accept(rws, tag, rel);
        prg = cix((unsigned*)(ws + fm.blk<P_CNT>(0)), mat ? C_KV + 4 * hh + 2 * (mat - 1) : C_Q + 2 * hh);
      }
      Q_WL(0); Q_BAR; Q_SCHED;
      const f32x4 a = *(lpa)(lb + s * 16384 + (t ^ 256) * 16) + (isV ? acc[2 * s][kh] : acc[2 * s + kh][0]);
      const f32x4 b = *(lpa)(lb + s * 16384 + 8192 + (t ^ 256) * 16) + (isV ? acc[2 * s + 1][kh] : acc[2 * s + kh][1]);
      put(s, kh ? od : 0u, u32x4{cvt2(a[0], a[1]), cvt2(a[2], a[3]), cvt2(b[0], b[1]), cvt2(b[2], b[3])});
      Q_SCHED;
    };
#pragma unroll
    for (int r = 0; r < 8; ++r) {
      asm volatile("s_waitcnt vmcnt(%1) lgkmcnt(0)\n s_barrier" : "+v"(du[0]) : "i"(r == 1 ? 12 : 4 + (r == 6 && !isO)) : "memory");
      if (r == 0) { stage(0); stage(1); }
      if (r == 5 && !isO) asm volatile("buffer_load_dwordx4 %0, %1, %2, 0 offen sc1" : "=v"(rel) : "v"(rl_off(tag.vid)), "s"(rws));
      if (r < 6) round(r + 2);
      else stage(9 - r);
#pragma unroll
      for (int n = 0; n < PFD; ++n) if (r == 7) ahead(n, n + 2);
      Q_SCHED;
#pragma unroll
      for (int i = 0; i < 4; ++i) {
        const s16x4 lo4 = __builtin_amdgcn_ds_read_tr16_b64_v4i16((__attribute__((address_space(3))) s16x4*)(pb[i & 1] + WBUF(r) + (i >> 1) * 8192));
        const s16x4 hi4 = __builtin_amdgcn_ds_read_tr16_b64_v4i16((__attribute__((address_space(3))) s16x4*)(pb[i & 1] + WBUF(r) + (i >> 1) * 8192 + 1024));
        wr[2 * r + (i >> 1)][i & 1] = bf16x8{lo4[0], lo4[1], lo4[2], lo4[3], hi4[0], hi4[1], hi4[2], hi4[3]};
      }
      Q_SCHED;
#pragma unroll
      for (int i = 0; i < 4; ++i) if (r == XR) fetchx(i);
      Q_SCHED;
#pragma unroll
      for (int u = 0; u < PBU; ++u) if (pro(r - 1) + u < pro(r)) kstep(pro(r - 1) + u);
    }
    Q_BAR;
    first(0);
    asm volatile("s_waitcnt vmcnt(%0)" :: "i"(PFD + 2 - isO) : "memory");
    Q_BAR; Q_SCHED;
#pragma unroll
    for (int i = 0; i < 4; ++i) fetchx(16 + i);
#pragma unroll
    for (int n = 0; n < 16; ++n) {
      if (n == PM) {
        asm volatile("s_waitcnt vmcnt(0)" : "+v"(du[0]), "+v"(du[1]), "+v"(du[2]), "+v"(du[3]) :: "memory");
        Q_BAR; Q_SCHED;
        if (t == 0) __hip_atomic_store(prg, 1u, __ATOMIC_RELAXED, __HIP_MEMORY_SCOPE_AGENT);
      }
      if (n >= PM && n < PM + 4) pair(0, 4 + ((n - PM) >> 1), 2 * ((n - PM) & 1));
      kstep(16 + n);
    }
    first(1);
    stage(6);
    Q_WV(5);
    Q_BAR; Q_SCHED;
  }
  static_assert(PS0 >= PSO && 2 * (NSEG_ - PSO) + 12 <= PSO, "in time");
  auto wipe = [&](int s) {
    const unsigned zb = mat ? fm.blk<P_VM>((unsigned)((2 - mat) * 512 + s * 8 + (hh >> 2))) + (unsigned)(hh & 3) * 2048u : fm.blk<P_O>((unsigned)(32 * s + (hh >> 1))) + (unsigned)(hh & 1) * 4096u;
    const unsigned zo = mat && w >= 2 ? 1u << 31 : (unsigned)tid() * 16u;
    asm volatile("v_mov_b64 v[252:253], -1\n v_mov_b64 v[254:255], -1\n s_nop 2\n buffer_store_dwordx4 v[252:255], %0, %1, %2 offen sc1\n buffer_store_dwordx4 v[252:255], %0, %1, %3 offen sc1\n s_nop 1"
                 :: "v"(zo), "s"(rws), "s"(zb), "s"(mat ? 1u << 31 : zb + 262144u) : "memory", "v252", "v253", "v254", "v255");
  };
  auto hand = [&](int d) {
#define HW(r, o) "ds_write_addtid_b32 %" #r " offset:" #o "\n"
    asm volatile("s_add_u32 m0, %8, %9\n s_nop 1\n" HW(0, 0) HW(1, 256) HW(2, 512) HW(3, 768) HW(4, 1024) HW(5, 1280) HW(6, 1536) HW(7, 1792)
                 :: "v"(acc[0][0][0]), "v"(acc[0][0][1]), "v"(acc[0][0][2]), "v"(acc[0][0][3]), "v"(acc[0][1][0]), "v"(acc[0][1][1]), "v"(acc[0][1][2]), "v"(acc[0][1][3]),
                    "s"(lbu + (unsigned)wc * 2048u), "i"((d & 1) * 8192) : "memory", "scc");
  };
  float ho[8];
  unsigned fv[8];
  auto fetch = [&](int d) {
#pragma unroll
    for (int i = 0; i < 8; ++i) ho[i] = *(const __attribute__((address_space(3))) float*)(lb + wc * 2048 + l * 4 + (d & 1) * 8192 + i * 256);
  };
  auto finish = [&](int s, int d) {
#pragma unroll
    for (int i = 0; i < 8; ++i) asm("v_add_f32 %0, %0, %1" : "+v"(ho[i]) : "v"(acc[0][i >> 2][i & 3]));
#pragma unroll
    for (int i = 0; i < 4; ++i) fv[isV ? (i & 2) * 2 + 2 * d + (i & 1) : i] = cvt2(ho[2 * i], ho[2 * i + 1]);
    const unsigned o = s < 2 ? 1u << 31 : 0u;
    if (!isV || d) put(s, o + (isV ? 0u : d * od), u32x4{fv[0], fv[1], fv[2], fv[3]});
  };
  auto second = [&](int s) { put(s, (s < 2 ? 1u << 31 : 0u) + od, u32x4{fv[4], fv[5], fv[6], fv[7]}); };
#pragma unroll
  for (int i = 0; i < 8; ++i) fv[i] = 0u;
#pragma unroll
  for (int i = 0; i < PD; ++i) if (!kh) frag(i);
#pragma nounroll
  for (int s2 = 2; s2 < NS; s2 += 2) {
#pragma unroll
    for (int d = 0; d < 4; ++d) {
      if (kh) {
        if ((d & 1) && s2 < SIG0) asm volatile("s_waitcnt vmcnt(%0)" :: "i"(2 * (qcnt(QSB, 99, '0') + qcnt(QSB, 99, '2') - qcnt(QSB, qpos(QSB, 's'), '0') - qcnt(QSB, qpos(QSB, 's'), '2'))) : "memory");
        if (d == 0 && s2 == 2) Q_WV(4);
        asm volatile("s_waitcnt vmcnt(%0)\n s_barrier" :: "i"(isV ? 4 + 2 * (d & 1) : 5) : "memory");
      } else {
        if (d == 0 && s2 == 2) Q_WV(4);
        asm volatile("s_waitcnt vmcnt(8) lgkmcnt(0)\n s_barrier" ::: "memory");
      }
      if (isO && (d & 1)) need(s2 + (d + 3) / 2);
      Q_SCHED;
#pragma unroll
      for (int x = 0; x < 32; ++x) {
        if (x >= qpos(kh ? QSB : QSA, 0)) continue;
        const char c = (kh ? QSB : QSA)[x];
        const int i = qcnt(kh ? QSB : QSA, x, '.');
        if (c == 'f') fetch(d + 1);
#pragma unroll
        for (int k = 0; k < PD; ++k) if (c == 'F') frag(16 * d + k);
        if (c == 'g' && t == 256 && ((d & 1) ? s2 < SIG0 : d == 0)) __hip_atomic_store(prg, (unsigned)max(s2 + (d >> 1) - (~d & 1), 2), __ATOMIC_RELAXED, __HIP_MEMORY_SCOPE_AGENT);
        if (c == '0' || c == '2') pair(s2, d + 3, c - '0');
        if (c == 'e') finish(s2 + ((d - 1) >> 1), ~d & 1);
        if (c == 's' && isV && !(d & 1)) second(s2 + ((d - 1) >> 1));
        if (c == 'h') hand(d);
        if (c == 'n') asm volatile("s_nop 7");
        if (c == '+') __builtin_amdgcn_s_setprio(1);
        if (c == '-') __builtin_amdgcn_s_setprio(0);
        if (c == '.' && (!kh || i + PD < 16)) frag(16 * d + i + PD);
        Q_SCHED;
#pragma unroll
        for (int j = 0; j < (c == '.' ? 2 : 0); ++j) {
          if (i) asm volatile("v_mfma_f32_16x16x32_bf16 %0, %1, %2, %0" : "+v"(acc[0][j]) : "v"(isV ? af[i & 7] : wr[i][j]), "v"(isV ? wr[i][j] : af[i & 7]));
          else asm volatile("v_mfma_f32_16x16x32_bf16 %0, %1, %2, 0" : "=&v"(acc[0][j]) : "v"(isV ? af[i & 7] : wr[i][j]), "v"(isV ? wr[i][j] : af[i & 7]));
        }
        Q_SCHED;
      }
      if (!isO && !isV && !kh && d == 2 && wop)
        asm volatile("s_mov_b32 m0, %3\n v_lshlrev_b32 v255, 3, %0\n buffer_load_dword v255, %1, %2 offen lds" :: "v"(voffA[0]), "s"(mkrsrc(wop, HD_ * D_ * 2u)), "s"((unsigned)(hh * 64 + s2) * 4096u),
                     "s"(lbu + 16384u) : "memory", "v255");
      if (!isO && !kh && d == 0 && s2 >= 8 && s2 < 8 + 2 * (NSEG_ - PSO)) wipe(PSO + (s2 >> 1) - 4);
    }
  }
  asm volatile("s_waitcnt lgkmcnt(0)\n s_barrier\n s_nop 7" : "+v"(acc[0][0]), "+v"(acc[0][1]) :: "memory");
  if (kh) { fetch(1); finish(NS - 1, 1); if (isV) second(NS - 1); }
  Q_WV(0); Q_BAR;
  if (t == 0) __hip_atomic_store(prg, (unsigned)NS, __ATOMIC_RELAXED, __HIP_MEMORY_SCOPE_AGENT);
#pragma nounroll
  for (int g = 6 * NOS + mat; isO && g < 2 * NSEG_; g += 3) {
    const unsigned so = fm.blk<P_O>((unsigned)(g * 16 + (hh >> 3) * 4));
    if (l == 0) cnt_wait<CSLF>(cnt, C_O + ((g >> 1) - 2) * 4 + (hh >> 3), 8u);
    u32x4 v[4];
#pragma nounroll
    for (int it = 0; it < WAIT_CAP; ++it) {
      asm volatile("" ::: "memory");
#pragma unroll
      for (int i = 0; i < 4; ++i) v[i] = __builtin_amdgcn_raw_buffer_load_b128(rws, voffA[1] + (unsigned)i * 1024u, so, 16);
      if (__all(max4(max4(max4(max4(0u, v[0]), v[1]), v[2]), v[3]) != ~0u)) break;
      __builtin_amdgcn_s_sleep(PSLO);
    }
#pragma unroll
    for (int i = 0; i < 4; ++i) *(lp4)(lb + RING + wc * 8192 + (kh ^ 1) * 1024 + i * 2048 + l * 16) = v[i];
    __syncthreads();
    f32x4 c0 = {0.f, 0.f, 0.f, 0.f}, c1 = c0;
#pragma unroll
    for (int i = 0; i < 16; ++i) {
      const bf16x8 a = *(lpf)(pa[i & 3] + (i >> 2) * FST);
      c0 = mfma16(wr[i][0], a, c0); c1 = mfma16(wr[i][1], a, c1);
    }
    if (!kh) { *(lpa)(lb + t * 32) = c0; *(lpa)(lb + t * 32 + 16) = c1; }
    __syncthreads();
    if (kh) {
      c0 += *(lpa)(lb + (t ^ 256) * 32); c1 += *(lpa)(lb + (t ^ 256) * 32 + 16);
      st_sc1(rws, on + lastp(g), u32x4{min(cvt2(c0[0], c0[1]), ~1u), min(cvt2(c0[2], c0[3]), ~1u), min(cvt2(c1[0], c1[1]), ~1u), min(cvt2(c1[2], c1[3]), ~1u)});
    }
  }
  return fm;
}

#define PSLP 4
#define ATW0 32
template <int N>
DEVI void mix(const int g0, const int dg, const int q, rsrc_t rws, const Far fm, unsigned* cnt, u16* __restrict__ out) {
  const int t = tid(), l = t & 63, w = __builtin_amdgcn_readfirstlane(t >> 6), c = 2 * q + (w >> 2);
  const unsigned col = (unsigned)(256 * q + 32 * w + 8 * (l >> 4));
  float gv[N];
  u32x4 bb[N], p[N][4];
#pragma unroll
  for (int i = 0; i < N; ++i) {
    const int g = g0 + i * dg;
    gv[i] = __uint_as_float(ld_sc1_32(rws, GATE(fm) + (unsigned)(16 * g + (l & 15)) * 4u));
    bb[i] = ld_sc1(rws, fm.blk<P_B>((unsigned)g * 4u) + ((unsigned)((l & 15) + (l & 12)) * D_ + col) * 2u);
  }
  {
    const int g = g0 + (l >> 2) * dg;
    if (l < 4 * N) cnt_wait(cnt, C_P + (g < 6 * NOS ? (g >> 1) % 3 : g % 3) * 32 + (l & 3) * 8 + c, g < 6 * NOS ? g / 6 + 1 : NOS - 1);
  }
#pragma nounroll
  for (int it = 0; it < WAIT_CAP; ++it) {
    asm volatile("" ::: "memory");
    unsigned mw = 0u;
#pragma unroll
    for (int i = 0; i < 4 * N; ++i) {
      const int g = g0 + (i >> 2) * dg;
      p[i >> 2][i & 3] = ld_sc1(rws, fm.blk<P_PT>((unsigned)((g >> 1) * 32 + (i & 3) * 8 + c)) + (unsigned)((g & 1) * 4096 + (w & 3) * 1024 + l * 16));
    }
    if (g0 < 6 * NOS) break;
#pragma unroll
    for (int i = 0; i < 4 * N; ++i) mw = max4(mw, p[i >> 2][i & 3]);
    if (__all(mw != ~0u)) break;
    __builtin_amdgcn_s_sleep(PSLP);
  }
#pragma unroll
  for (int i = 0; i < N; ++i) {
    const float g = gv[i], omg = rbf(1.0f - g);
    u32x4 r;
#pragma unroll
    for (int k = 0; k < 4; ++k) {
      const unsigned av = cvt2((__uint_as_float(p[i][0][k] << 16) + __uint_as_float(p[i][1][k] << 16)) + (__uint_as_float(p[i][2][k] << 16) + __uint_as_float(p[i][3][k] << 16)),
                               (__uint_as_float(p[i][0][k] & 0xffff0000u) + __uint_as_float(p[i][1][k] & 0xffff0000u)) + (__uint_as_float(p[i][2][k] & 0xffff0000u) + __uint_as_float(p[i][3][k] & 0xffff0000u)));
      const unsigned t1 = cvt2(g * __uint_as_float(av << 16), g * __uint_as_float(av & 0xffff0000u));
      const unsigned t2 = cvt2(omg * __uint_as_float(bb[i][k] << 16), omg * __uint_as_float(bb[i][k] & 0xffff0000u));
      r[k] = cvt2(__uint_as_float(t1 << 16) + __uint_as_float(t2 << 16), __uint_as_float(t1 & 0xffff0000u) + __uint_as_float(t2 & 0xffff0000u));
    }
    __builtin_amdgcn_raw_buffer_store_b128(r, mkrsrc(out, S_ * D_ * 2u), ((unsigned)(16 * (g0 + i * dg) + (l & 15)) * D_ + col) * 2u, 0, 0);
  }
}

#define EROW(te) (32 * ((te) >> 1) + 8 * (lo >> 2) + 4 * ((te) & 1) + (lo & 3))
template <bool BULK>
DEVI void xattn_wave(const int l, const int s0, const int ds, const int se, const int h, const char* ws, rsrc_t rw, const Far fm, unsigned* cnt) {
  const int g = l >> 4, lo = l & 15;
  constexpr float CL = 0.12751743f, MASKED = -1.4426950e9f;
  auto ld = [&](unsigned v, unsigned so) { return __builtin_amdgcn_raw_buffer_load_b128(rw, v, so, 16); };
  const unsigned okm = (unsigned)((g * 8 + (lo & 7)) * 16), ovm = (unsigned)(EROW(g >> 1) * 16 + 8 * (g & 1));
  u32x4 q[8], k[8], v[8], km[4];
  u32x2 vm[4];
  auto ldq = [&](int sh) {
#pragma unroll
    for (int i = 0; i < 8; ++i) q[i] = ld((unsigned)(l * 16 + (i & 3) * 1024), fm.blk<P_Q>(sh) + (i >> 2) * 4096u);
  };
  auto ldk = [&](int sh) {
#pragma unroll
    for (int i = 0; i < 8; ++i) k[i] = ld((unsigned)(l * 16 + (i & 3) * 1024), (fm.blk<P_K>(sh) ^ KVX) + (i >> 2) * 4096u);
  };
  auto ldv = [&](int sh) {
#pragma unroll
    for (int i = 0; i < 8; ++i) v[i] = ld((unsigned)(l * 16 + (i & 3) * 1024), (fm.blk<P_VX>(sh) ^ KVX) + (i >> 2) * 4096u);
  };
  auto ldkm = [&](int sh) {
#pragma unroll
    for (int i = 0; i < 4; ++i) km[i] = ld(okm + (unsigned)i * 512u, fm.blk<P_KM>(sh >> 2) + (sh & 3) * 2048u);
  };
  auto ldvm = [&](int sh) {
#pragma unroll
    for (int i = 0; i < 4; ++i) vm[i] = __builtin_amdgcn_raw_buffer_load_b64(rw, ovm + (unsigned)i * 512u, fm.blk<P_VM>(sh >> 2) + (sh & 3) * 2048u, 16);
  };
  auto mm = [](u32x4 a, u32x4 b, f32x4 c) { return mfma16(__builtin_bit_cast(bf16x8, a), __builtin_bit_cast(bf16x8, b), c); };
  const f32x4 z4 = f32x4{0.f, 0.f, 0.f, 0.f};
  { const int sh = s0 * H_ + h; ldq(sh); ldk(sh); ldv(sh); if (BULK) { ldkm(sh); ldvm(sh); } }
#pragma nounroll
  for (int s = s0; s < se; s += ds) {
    const int sh = s * H_ + h, sn = sh + ds * H_;
    const bool more = s + ds < se;
    f32x4 o[8][2];
    float m1[2], s1[2];
    u32x4 pb[2];
    {
      f32x4 st[2][2];
#pragma unroll
      for (int i = 0; i < 16; ++i) st[i & 1][(i >> 1) & 1] = mm(k[(i & 1) * 4 + (i >> 2)], q[(i & 2) * 2 + (i >> 2)], i < 4 ? z4 : st[i & 1][(i >> 1) & 1]);
      Q_SCHED;
      if (more) ldk(sn);
      Q_SCHED;
#pragma unroll
      for (int n = 0; n < 2; ++n) {
        float y[8];
#pragma unroll
        for (int i = 0; i < 8; ++i) y[i] = 8 * g + 2 * (i & 2) + 2 * (i >> 2) + (i & 1) <= 16 * n + lo ? st[i >> 2][n][i & 3] * CL : MASKED;
        m1[n] = rows_max(fmx3(fmx3(y[0], y[1], y[2]), fmx3(y[3], y[4], y[5]), fmx(y[6], y[7])));
#pragma unroll
        for (int i = 0; i < 8; ++i) y[i] = __builtin_amdgcn_exp2f(y[i] - m1[n]);
        s1[n] = ((y[0] + y[1]) + (y[2] + y[3])) + ((y[4] + y[5]) + (y[6] + y[7]));
        pb[n] = u32x4{cvt2(y[0], y[1]), cvt2(y[4], y[5]), cvt2(y[2], y[3]), cvt2(y[6], y[7])};
      }
    }
#pragma unroll
    for (int i = 0; i < 16; ++i) o[i >> 1][i & 1] = mm(v[i >> 1], pb[i & 1], z4);
    Q_SCHED;
    if (more) ldv(sn);
    if (s > s0) {
      if (more) asm volatile("s_waitcnt vmcnt(16)" ::: "memory");
      else drain_vm();
      if (l == 0) cnt_add(cnt, C_O + (s - ds) * 4 + (h >> 3));
    }
    if (!BULK) {
      cnt_wait<CSLL>(cnt, C_MEM + sh - 3 * H_, 4u);
      const unsigned tw = clk32();
      cnt_wait<CSLF>(cnt, C_MEM + (s >= PS0 ? sh - 2 * H_ : sh), 4u);
      if (s >= PS0 && clk32() - tw > 2000u) __builtin_amdgcn_s_sleep(ATW0);
      for (int it = 0; it < WAIT_CAP; ++it) {
        asm volatile("" ::: "memory");
        ldkm(sh); ldvm(sh);
        if (s < PS0) break;
        const unsigned mw = max4(max4(max4(max4(max4(max4(0u, km[0]), km[1]), km[2]), km[3]), u32x4{vm[0][0], vm[0][1], vm[1][0], vm[1][1]}), u32x4{vm[2][0], vm[2][1], vm[3][0], vm[3][1]});
        if (__all(mw != ~0u)) break;
        __builtin_amdgcn_s_sleep(PSLP);
      }
    }
    Q_SCHED;
    f32x4 sm[2];
#pragma unroll
    for (int i = 0; i < 8; ++i) sm[i & 1] = mm(km[i >> 1], q[(i & 1) * 4 + (i >> 1)], i < 2 ? z4 : sm[i & 1]);
    Q_SCHED;
    if (more) { ldq(sn); if (BULK) ldkm(sn); }
    Q_SCHED;
    s16x4 pm[2][2];
    float f[2];
#pragma unroll
    for (int n = 0; n < 2; ++n) {
      f32x4 y = sm[n] * CL;
      const unsigned u = __float_as_uint(fmx3(fmx3(y[0], y[1], y[2]), y[3], m1[n]));
      const auto sw = __builtin_amdgcn_permlane16_swap(u, u, false, false);
      const float m = fmx(__uint_as_float(sw[0]), __uint_as_float(sw[1])), a1 = __builtin_amdgcn_exp2f(m1[n] - m);
#pragma unroll
      for (int i = 0; i < 4; ++i) y[i] = __builtin_amdgcn_exp2f(y[i] - m);
      const float inv = __builtin_amdgcn_rcpf(rows_sum(a1 * s1[n] + (g < 2 ? (y[0] + y[1]) + (y[2] + y[3]) : 0.f)));
      const u32x2 p = u32x2{cvt2(y[0] * inv, y[1] * inv), cvt2(y[2] * inv, y[3] * inv)};
      pm[0][n] = __builtin_bit_cast(s16x4, g < 2 ? p : u32x2{0u, 0u});
      pm[1][n] = __builtin_bit_cast(s16x4, g < 2 ? u32x2{0u, 0u} : p);
      f[n] = a1 * inv;
    }
#pragma unroll
    for (int i = 0; i < 16; ++i)
      o[i >> 1][i & 1] = __builtin_amdgcn_mfma_f32_16x16x16bf16_1k(__builtin_bit_cast(s16x4, vm[i >> 2]), pm[(i >> 1) & 1][i & 1], o[i >> 1][i & 1] * f[i & 1], 0, 0, 0);
    Q_SCHED;
    if (BULK && more) ldvm(sn);
    Q_SCHED;
#pragma unroll
    for (int i = 0; i < 8; ++i) {
      const f32x4 a = o[i & 6][i & 1], b = o[(i & 6) + 1][i & 1];
      st_sc1(rw, fm.blk<P_O>(32 * s + (h >> 1)) + (unsigned)((i & 1) * 256 + 4 * (h & 1) + (i >> 1)) * 1024u + (unsigned)l * 16u,
             u32x4{min(cvt2(a[0], a[1]), ~1u), min(cvt2(a[2], a[3]), ~1u), min(cvt2(b[0], b[1]), ~1u), min(cvt2(b[2], b[3]), ~1u)});
    }
    Q_SCHED;
    if (!more) { drain_vm(); if (l == 0) cnt_add(cnt, C_O + s * 4 + (h >> 3)); }
  }
}

typedef float f32x2 __attribute__((ext_vector_type(2)));
typedef __attribute__((address_space(3))) f32x2* lpc;
constexpr int FROW = 32 * 264 + 16;
constexpr int FFT_LDS = 16 * FROW + 16384;
#define PKM(xh) " op_sel:[" #xh ",%4,0] op_sel_hi:[" #xh ",%5,1] neg_lo:[0,%6,0] neg_hi:[0,%7,0]"
#define PKFMA(C, xh) asm("v_pk_fma_f32 %0, %1, %2, %3" PKM(xh) : "=v"(r) : "v"(x), C(w), "v"(c), "i"(sl), "i"(1 - sl), "i"(nl), "i"(nh))
template <bool SG> DEVI f32x2 pf0(f32x2 x, f32x2 w, f32x2 c, int sl, int nl, int nh) { f32x2 r; if (SG) PKFMA("s", 0); else PKFMA("v", 0); return r; }
template <bool SG> DEVI f32x2 pf1(f32x2 x, f32x2 w, f32x2 c, int sl, int nl, int nh) { f32x2 r; if (SG) PKFMA("s", 1); else PKFMA("v", 1); return r; }
template <bool SG> DEVI f32x2 cmul(f32x2 x, f32x2 w) {
  f32x2 t;
  if (SG) asm("v_pk_mul_f32 %0, %1, %2 op_sel_hi:[0,1]" : "=v"(t) : "v"(x), "s"(w));
  else asm("v_pk_mul_f32 %0, %1, %2 op_sel_hi:[0,1]" : "=v"(t) : "v"(x), "v"(w));
  return pf1<SG>(x, w, t, 1, 1, 0);
}
DEVI f32x2 addx(f32x2 a, f32x2 b, int nl, int nh) {
  f32x2 r;
  asm("v_pk_add_f32 %0, %1, %2 op_sel:[0,1] op_sel_hi:[1,0] neg_lo:[0,%3] neg_hi:[0,%4]" : "=v"(r) : "v"(a), "v"(b), "i"(nl), "i"(nh));
  return r;
}
template <bool INV, bool SG> DEVI void bfly(f32x2& a, f32x2& b, f32x2 w, int rot) {
  const int sg = rot ^ (int)INV;
  const f32x2 u = pf1<SG>(b, w, pf0<SG>(b, w, a, rot, 0, sg), 1 - rot, 1 - sg, 0);
  b = __builtin_elementwise_fma(a, f32x2{2.f, 2.f}, -u);
  a = u;
}
template <bool INV> DEVI void bfly1(f32x2& a, f32x2& b, int rot) {
  const f32x2 u = rot ? addx(a, b, INV, !INV) : a + b;
  b = rot ? addx(a, b, !INV, INV) : a - b;
  a = u;
}
DEVI f32x2 w32(int k) {
  constexpr float c[9] = {1.f, 0.98078528040323044913f, 0.92387953251128675613f, 0.83146961230254523708f, 0.70710678118654752440f,
                          0.55557023301960222474f, 0.38268343236508977173f, 0.19509032201612826785f, 0.f};
  return f32x2{c[k], -c[8 - k]};
}
template <bool INV, bool SHIFT>
DEVI void dft32(f32x2 (&v)[32], const f32x2 (&be)[5]) {
  f32x2 x[32];
#pragma unroll
  for (int i = 0; i < 32; ++i) x[i] = v[((i & 1) << 4) | ((i & 2) << 2) | (i & 4) | ((i & 8) >> 2) | (i >> 4)];
#pragma unroll
  for (int j = 0; j < 5; ++j) {
    const int H = 1 << j, Q = j ? H >> 1 : 1;
    f32x2 tw[8];
    if (SHIFT) {
      tw[0] = be[j];
#pragma unroll
      for (int k = 1; k < Q; ++k) tw[k] = cmul<true>(be[j], w32(k << (4 - j)));
    }
#pragma unroll
    for (int i = 0; i < 16; ++i) {
      const int k = i & (H - 1), a = ((i >> j) << (j + 1)) + k, rot = k >= Q, kk = k - rot * Q;
      if (SHIFT) bfly<INV, false>(x[a], x[a + H], tw[kk], rot);
      else if (kk == 0) bfly1<INV>(x[a], x[a + H], rot);
      else bfly<INV, true>(x[a], x[a + H], w32(kk << (4 - j)), rot);
    }
  }
#pragma unroll
  for (int i = 0; i < 32; ++i) v[i] = x[i];
}
DEVI void ldtw(f32x2 (&be)[5], rsrc_t rw, int m) {
#pragma unroll
  for (int j = 0; j < 5; ++j) be[j] = __builtin_bit_cast(f32x2, ld_sc1_64(rw, W_TW + (unsigned)(j * 512 + m * 8)));
}
DEVI void xchg(f32x2 (&v)[32], ldsp reg, int j) {
#pragma unroll
  for (int i = 0; i < 32; ++i) *(lpc)(reg + i * 264 + j * 8) = v[i];
  __builtin_amdgcn_wave_barrier();
#pragma unroll
  for (int i = 0; i < 32; ++i) v[i] = *(lpc)(reg + j * 264 + i * 8);
  __builtin_amdgcn_wave_barrier();
}
DEVI unsigned nbr(unsigned v) { return __builtin_amdgcn_mov_dpp(v, 0xb1, 0xf, 0xf, true); }
DEVI void swap32(f32x2& a, f32x2& b) {
#pragma unroll
  for (int i = 0; i < 2; ++i) {
    const auto s = __builtin_amdgcn_permlane32_swap(__float_as_uint(a[i]), __float_as_uint(b[i]), false, false);
    a[i] = __uint_as_float(s[0]); b[i] = __uint_as_float(s[1]);
  }
}

DEVI void fft_rows_fwd(char* lds, const int blk, const u16* __restrict__ x, const char* ws, rsrc_t rw, const Far fm) {
  const int t = tid(), j = t & 31, hf = t >> 5;
  const ldsp lb = (ldsp)lds, reg = lb + hf * FROW;
  const unsigned* xr = (const unsigned*)(x + (size_t)(16 * blk + hf + (j & 1) * 1024) * D_) + (j >> 1);
  unsigned u[32];
#pragma unroll
  for (int i = 0; i < 32; ++i) u[i] = ldg<(FNT & 4) != 0>(xr + 16 * i);
  f32x2 be[5], v[32];
  ldtw(be, rw, 2 * j);
  const unsigned sr = (j & 1) ? 0x03020c0cu : 0x05040c0cu, si = (j & 1) ? 0x07060c0cu : 0x01000c0cu;
#pragma unroll
  for (int i = 0; i < 32; ++i) {
    const unsigned n = nbr(u[i]);
    v[i] = f32x2{__uint_as_float(__builtin_amdgcn_perm(u[i], n, sr)), __uint_as_float(__builtin_amdgcn_perm(u[i], n, si))};
  }
  dft32<false, false>(v, be);
  xchg(v, reg, j);
  dft32<false, true>(v, be);
#pragma unroll
  for (int i = 0; i < 32; ++i) *(lpc)(reg + i * 256 + j * 8) = v[i];
  __syncthreads();
  const int k0 = t >> 4;
  const ldsp pr = lb + (t & 15) * FROW;
#pragma unroll
  for (int i = 0; i < 16; ++i) {
    const f32x2 a = *(lpc)(pr + k0 * 8 + i * 256), b = *(lpc)(pr + ((i == 0 && k0 == 0) ? 4096 : 8192 - k0 * 8 - i * 256));
    st_sc1(rw, fm.blk<P_T1>(64 * i + (blk >> 5)) + (unsigned)(k0 * 2048 + 16 * (blk & 31) + (t & 15)) * 16u, __builtin_bit_cast(u32x4, f32x4{a[0], a[1], b[0], b[1]}));
  }
}
DEVI void fft_cols(char* lds, const int blk, const char* ws, rsrc_t rw, const Far fm) {
  const int t = tid(), l = t & 63, j = t & 31, u = (t >> 5) & 1, k = 8 * blk + __builtin_amdgcn_readfirstlane(t >> 6);
  const ldsp lb = (ldsp)lds, reg = lb + (t >> 5) * FROW;
  f32x4 z[16];
#pragma unroll
  for (int i = 0; i < 16; ++i) { z[i] = __builtin_bit_cast(f32x4, ld_sc1(rw, fm.blk<P_T1>(2 * k) + (unsigned)(64 * (i + (i & 8)) + l) * 16u)); }
  f32x2 b0[5], b1[5], b2[5], v[32];
  ldtw(b0, rw, 32 * u);
  ldtw(b1, rw, 2 * j + u);
  u32x4 f[8];
  const unsigned fo = (unsigned)(2 * j + u) * 16u;
#pragma unroll
  for (int i = 0; i < 8; ++i) { f[i] = ld_sc1(rw, fo + fm.blk<P_FT>(k) + (unsigned)(i * 1024)); }
  ldtw(b2, rw, 2 * j);
  const f32x2 bl = __builtin_bit_cast(f32x2, ld_sc1_64(rw, W_TW + (unsigned)(4 * 512 + l * 8)));
#pragma unroll
  for (int i = 0; i < 16; ++i) {
    f32x2 a = f32x2{z[i][0] + z[i][2], z[i][1] - z[i][3]}, b = f32x2{z[i][1] + z[i][3], z[i][2] - z[i][0]};
    if (k == 0) { a = f32x2{z[i][0], z[i][2]}; b = f32x2{z[i][1], z[i][3]}; }
    v[2 * i] = a + b; v[2 * i + 1] = a - b;
    swap32(v[2 * i], v[2 * i + 1]);
  }
  dft32<false, true>(v, b0);
  xchg(v, reg, j);
  dft32<false, true>(v, b1);
  auto unp = [](unsigned w) { return f32x2{__uint_as_float(w << 16), __uint_as_float(w & 0xffff0000u)}; };
  if (k == 0) {
    const ldsp pf = lb + 16 * FROW;
#pragma unroll
    for (int i = 0; i < 8; ++i) {
      const u32x4 g = ld_sc1(rw, fo + fm.blk<P_FT>(512) + (unsigned)(i * 1024));
#pragma unroll
      for (int e = 0; e < 4; ++e) *(lp2)(pf + (2 * j + u) * 8 + (4 * i + e) * 512) = u32x2{f[i][e], g[e]};
    }
#pragma unroll
    for (int i = 0; i < 32; ++i) *(lpc)(lb + (2 * j + u) * 8 + i * 512) = v[i];
    __builtin_amdgcn_wave_barrier();
#pragma nounroll
    for (int s = l; s <= 1024; s += 64) {
      const int m = (2048 - s) & 2047;
      const f32x2 a = *(lpc)(lb + s * 8), b = *(lpc)(lb + m * 8);
      const u32x2 fa = *(lp2)(pf + s * 8), fb = *(lp2)(pf + m * 8);
      const f32x2 cj = f32x2{1.f, -1.f};
      const f32x2 w0 = 0.5f * (unp(fa[0]) + cj * unp(fb[0])), w5 = 0.5f * (unp(fa[1]) + cj * unp(fb[1]));
      const f32x2 c0 = cmul<false>(a + cj * b, w0), c5 = cmul<false>(a - cj * b, w5);
      *(lpc)(lb + s * 8) = c0 + c5;
      *(lpc)(lb + m * 8) = cj * (c0 - c5);
    }
    __builtin_amdgcn_wave_barrier();
#pragma unroll
    for (int i = 0; i < 32; ++i) v[i] = *(lpc)(lb + (2 * j + u) * 8 + i * 512);
    __builtin_amdgcn_wave_barrier();
  } else {
#pragma unroll
    for (int i = 0; i < 32; ++i) v[i] = cmul<false>(v[i], unp(f[i >> 2][i & 3]));
  }
  dft32<true, false>(v, b2);
  xchg(v, reg, j);
  dft32<true, true>(v, b2);
#pragma unroll
  for (int i = 0; i < 16; ++i) {
    swap32(v[2 * i], v[2 * i + 1]);
    bfly<true, false>(v[2 * i], v[2 * i + 1], (i & 7) ? cmul<true>(bl, w32(i & 7)) : bl, i >> 3);
    const f32x2 a = v[2 * i], b = v[2 * i + 1];
    f32x4 o = f32x4{a[0] - b[1], a[1] + b[0], a[0] + b[1], b[0] - a[1]};
    if (k == 0) o = f32x4{a[0], b[0], a[1], b[1]};
    st_sc1(rw, fm.blk<P_T2>(2 * k) + (unsigned)(64 * (i + (i & 8)) + l) * 16u, __builtin_bit_cast(u32x4, o));
  }
}
DEVI void fft_rows_inv(char* lds, const int blk, const char* ws, rsrc_t rw, const Far fm) {
  const int t = tid(), j = t & 31, hf = t >> 5, k0 = t >> 4;
  const ldsp lb = (ldsp)lds, reg = lb + hf * FROW, pr = lb + (t & 15) * FROW;
  f32x4 z[16];
#pragma unroll
  for (int i = 0; i < 16; ++i) { z[i] = __builtin_bit_cast(f32x4, ld_sc1(rw, fm.blk<P_T2>(64 * i + (blk >> 5)) + (unsigned)(k0 * 2048 + 16 * (blk & 31) + (t & 15)) * 16u)); }
  f32x2 be[5], v[32];
  ldtw(be, rw, 2 * j);
#pragma unroll
  for (int i = 0; i < 16; ++i) {
    *(lpc)(pr + k0 * 8 + i * 256) = f32x2{z[i][0], z[i][1]};
    *(lpc)(pr + ((i == 0 && k0 == 0) ? 4096 : 8192 - k0 * 8 - i * 256)) = f32x2{z[i][2], z[i][3]};
  }
  __syncthreads();
#pragma unroll
  for (int i = 0; i < 32; ++i) v[i] = *(lpc)(reg + i * 256 + j * 8);
  __builtin_amdgcn_wave_barrier();
  dft32<true, false>(v, be);
  xchg(v, reg, j);
  dft32<true, true>(v, be);
  const float sc = 0.5f / ((float)S_ * (float)D_);
  const unsigned sel = (j & 1) ? 0x07060302u : 0x01000504u;
#pragma unroll
  for (int i = 0; i < 32; ++i) {
    const unsigned c = cvt2(v[i][0] * sc, v[i][1] * sc);
    *(lp1)(reg + (j & 1) * 2112 + (j >> 1) * 4 + i * 64) = __builtin_amdgcn_perm(c, nbr(c), sel);
  }
  __builtin_amdgcn_wave_barrier();
#pragma unroll
  for (int i = 0; i < 8; ++i) {
    st_sc1(rw, fm.blk<P_B>(4 * blk + (i >> 2) * 256) + (unsigned)((hf + (hf & 12)) * D_ * 2 + (i & 3) * 512 + j * 16), *(lp4)(reg + (i >> 2) * 2112 + (i & 3) * 512 + j * 16));
  }
}
DEVI Far filter_gate(char* lds, const int un, const u16* __restrict__ wr, const u16* __restrict__ wi, const u16* __restrict__ x, const u16* __restrict__ gw,
                     rsrc_t rw, const Tag& tag) {
  const int t = tid(), l = t & 63;
  const ldsp lb = (ldsp)lds;
  u32x4 v[17], xv[8][2], wv[2];
#pragma unroll
  for (int i = 0; i < 17; ++i) {
    const int g = i < 16 ? i >> 1 : (t >> 1) & 7;
    v[i] = ldg<(FNT & 1) != 0>((const u32x4*)((const char*)((g & 4) ? wi : wr) + (size_t)((un & 3) + 16 * (un >> 2) + 4 * (g & 3)) * 16416 + (i < 16 ? t + 512 * (i & 1) : 1024 + (t & 1)) * 16));
  }
#pragma unroll
  for (int i = 0; i < 16; ++i) xv[i >> 1][i & 1] = ldg<(FNT & 2) != 0>((const u32x4*)(x + (size_t)(un * 64 + (t >> 6) * 8 + (i >> 1)) * D_ + (l + 64 * (i & 1)) * 8));
#pragma unroll
  for (int i = 0; i < 2; ++i) wv[i] = *(const u32x4*)(gw + (l + 64 * i) * 8);
  __builtin_amdgcn_sched_barrier(0);
#pragma unroll
  for (int i = 0; i < 17; ++i) *(lp4)(lb + (i < 16 ? (i >> 1) * 16416 + (i & 1) * 8192 + t * 16 : ((t >> 1) & 7) * 16416 + 16384 + (t & 1) * 16)) = v[i];
  __builtin_amdgcn_sched_barrier(0);
  const u32x4 rel = ld_sc1(rw, rl_off(tag.vid));
  __builtin_amdgcn_sched_barrier(0);
  float acc[8];
#pragma unroll
  for (int u = 0; u < 8; ++u) {
    acc[u] = 0.f;
#pragma unroll
    for (int k = 0; k < 8; ++k) {
      const unsigned a = xv[u][k >> 2][k & 3], b = wv[k >> 2][k & 3];
      const unsigned p = cvt2(__uint_as_float(a << 16) * __uint_as_float(b << 16), __uint_as_float(a & 0xffff0000u) * __uint_as_float(b & 0xffff0000u));
      acc[u] += __uint_as_float(p << 16);
      acc[u] += __uint_as_float(p & 0xffff0000u);
    }
  }
#pragma unroll
  for (int o = 32; o >= 1; o >>= 1)
#pragma unroll
    for (int u = 0; u < 8; ++u) acc[u] += __int_as_float(__builtin_amdgcn_ds_bpermute((l ^ o) * 4, __float_as_int(acc[u])));
#pragma unroll
  for (int o = 4; o >= 1; o >>= 1)
#pragma unroll
    for (int u = 0; u < o; ++u) acc[u] = (l & o) ? acc[u + o] : acc[u];
  if (l < 8) *(__attribute__((address_space(3))) float*)(lb + 8 * 16416 + (t >> 6) * 32 + l * 4) = rbf(1.0f / (1.0f + expf(-rbf(acc[0]))));
  const Far fm = accept(rw, tag, rel);
  __syncthreads();
  if (t < 16) st_sc1(rw, GATE(fm) + (unsigned)(un * 256 + t * 16), *(lp4)(lb + 8 * 16416 + t * 16));
  typedef __attribute__((address_space(3))) u16* lph;
  const int fw = __builtin_amdgcn_readfirstlane(t);
#pragma unroll
  for (int i = 0; i < 17; ++i) {
    const int f = i < 16 ? (t & ~15) + i : 512;
    const ldsp p = lb + ((t & 15) * FB_ + f) * 2;
    u32x4 o;
#pragma unroll
    for (int e = 0; e < 4; ++e) o[e] = pack2(*(lph)(p + e * 16416), *(lph)(p + (4 + e) * 16416));
    st_sc1(rw, (i < 16 || t < 16) ? fm.blk<P_FT>(i < 16 ? fw : 512) + (unsigned)((f & 63) * 16384 + (un >> 2) * 1024 + (16 * (un & 3) + (t & 15)) * 16) : ~0u, o);
  }
  return fm;
}

constexpr int SPIN_CAP = 1 << 17;
#define X1I 440
#define X2I 540
#define X3I 720
#define X0E 96
#define M1 16
#define M2 8
#define M3 20
#define RUP 1
#define RDN 2
#define RLIM 32
#define RUPL 24
#define RDNL 48
#define TSE 1
#define TS0 3
#define TA0 230
#define SLOT 1024u
#define LEADL -40
#define DMIN 170
#define DLYV 310
#define CK2 2880
#define KVL 0
#define BUMP1 8
#define BUMP2 32
#define DLYA 2
#define LEADF -40
#define E2 60u
#define TOL2 200u
#define DIAGT 30000u
DEVI unsigned now10() { return (unsigned)__builtin_amdgcn_s_memrealtime(); }
typedef unsigned u32x8 __attribute__((ext_vector_type(8)));
DEVI u32x2 ldtr(ldsp p) { return __builtin_bit_cast(u32x2, __builtin_amdgcn_ds_read_tr16_b64_v4i16((__attribute__((address_space(3))) s16x4*)p)); }
DEVI void dma1(unsigned voff, rsrc_t r, unsigned so, unsigned m) {
  asm volatile("s_mov_b32 m0, %3\n s_nop 3\n buffer_load_dwordx4 %0, %1, %2 offen sc1 lds" :: "v"(voff), "s"(r), "s"(so), "s"(m) : "memory");
}
typedef unsigned short u16x2 __attribute__((ext_vector_type(2)));
DEVI unsigned pkmax(unsigned a, unsigned b) { return __builtin_bit_cast(unsigned, __builtin_elementwise_max(__builtin_bit_cast(u16x2, a), __builtin_bit_cast(u16x2, b))); }
DEVI unsigned mag4(unsigned m, u32x4 v) { return pkmax(pkmax(m, pkmax(v[0] & 0x7fff7fffu, v[1] & 0x7fff7fffu)), pkmax(v[2] & 0x7fff7fffu, v[3] & 0x7fff7fffu)); }
DEVI bool missing(unsigned m) { return pkmax(m, 0x7f817f81u) != 0x7f817f81u; }
DEVI unsigned settle(unsigned v) {
  if ((v & 0x7fffu) > 0x7f80u) v = (v & 0xffff0000u) | 0x7f81u;
  if ((v & 0x7fff0000u) > 0x7f800000u) v = (v & 0xffffu) | 0x7f810000u;
  return v;
}
DEVI unsigned slots(unsigned t, unsigned tmin, unsigned sl) {
  int d = (int)(__builtin_amdgcn_readfirstlane(tmin) - __builtin_amdgcn_readfirstlane(t));
  unsigned k = 0u;
#pragma nounroll
  for (; d > 0 && k < 1023u; d -= (int)sl) { ++k; asm volatile("" : "+s"(k)); }
  return __builtin_amdgcn_readfirstlane(k);
}
DEVI unsigned scalar(unsigned v) { unsigned r; asm("s_nop 0\n v_readfirstlane_b32 %0, %1" : "=s"(r) : "v"(v)); return r; }
DEVI unsigned vec(unsigned v) { unsigned r; asm("s_nop 1\n v_mov_b32 %0, %1" : "=v"(r) : "s"(v)); return r; }
DEVI unsigned evens(unsigned a, unsigned b) { return __builtin_amdgcn_perm(b, a, 0x05040100u); }
DEVI unsigned odds(unsigned a, unsigned b) { return __builtin_amdgcn_perm(b, a, 0x07060302u); }
constexpr int TRY_CAP = 1 << 16;
#define VR8(p, a, b, c, d, e, f, g, h) "v" #p #a, "v" #p #b, "v" #p #c, "v" #p #d, "v" #p #e, "v" #p #f, "v" #p #g, "v" #p #h
#define VR10(p) VR8(p, 0, 1, 2, 3, 4, 5, 6, 7), "v" #p "8", "v" #p "9"
#define BAR() asm volatile("s_waitcnt lgkmcnt(0)\n s_barrier" ::: "memory")
#define SCHED __builtin_amdgcn_sched_barrier(0)
#define UNLIKELY(x) __builtin_expect(!!(x), 0)
#define RFL(x) ((unsigned)__builtin_amdgcn_readfirstlane(x))
constexpr int L_MEM = 0;
constexpr int L_QK = 16384;
constexpr int L_KV = L_QK + 4096;
constexpr int L_OWN = L_KV + 32768;
constexpr int L_J = L_OWN + 2048;
constexpr int L_T1 = 61952;
constexpr int L_KF = L_T1 + 16;
constexpr int L_A = L_T1 + 32;
constexpr int L_V = L_T1 + 48;
constexpr int L_TA = L_T1 + 64;
constexpr int L_DUM = L_TA + 16;
constexpr int L_EN = L_DUM + 256;
constexpr int L_TR = L_EN + 256;
constexpr int L_RP = L_J + 4096;
constexpr int L_FM = L_TR + 4096;
constexpr int L_WO = 73728, L_M0 = L_WO + 65536;
constexpr int LDS_BYTES = 163840;
static_assert(L_MEM == 0 && L_M0 % 1024 == 0 && L_RP + 1024 <= L_T1 && L_FM + 16 <= L_WO && L_WO + 65536 <= 147456 && GEMM_LDS <= LDS_BYTES && FFT_LDS <= LDS_BYTES && 32 * 1032 * 2 <= LDS_BYTES, "LDS");
#define LWJ
#define LWD
#define FLG
constexpr int JW = 1;
template <bool QK>
DEVI void recurrence(const int vid, const int wv, char* lds, const u16* __restrict__ mem0, const u16* __restrict__ Wq, const u16* __restrict__ Wk,
                     const u16* __restrict__ Wv, const u16* __restrict__ Wo, char* ws, const Tag& tag) {
  const ldsp lb = (ldsp)lds;
  const unsigned lbu = (unsigned)(size_t)lb;
  int l;
  asm volatile("v_mbcnt_lo_u32_b32 %0, -1, 0\n v_mbcnt_hi_u32_b32 %0, -1, %0" : "=v"(l));
  const int t = wv * 64 + l, w = wv & 3, g = l >> 4, lo = l & 15;
  const int h = vid >> 2, jj = vid & 3, vj = jj ^ 2;
  const rsrc_t rw = mkrsrc(ws, W_END);
  bool abort_ = false;
  u32x2 m0v[4];
#pragma unroll
  for (int i = 0; i < 4; ++i)
    asm volatile("s_nop 4\n buffer_load_dwordx2 %0, %1, %2, 0 offen offset:%3" : "=v"(m0v[i]) : "v"((unsigned)(((t >> 2) & 7) * D_ + (t >> 5) * 32 + (t & 2) * 8 + (t & 1) * 4) * 2u), "s"(mkrsrc(mem0, MEM_ * D_ * 2u)),
                 "i"((i >> 1) * 1024 + (i & 1) * 16));

  u32x8 w1[16];
  u32x4 w2[16];
  u32x4 rel;
    u16* stg = (u16*)lds;
    constexpr int SSTR = 40;
    u32x4 v[4][6];
#define RNT "nt"
#define ONT ""
#define WOA 8
    const rsrc_t ra = mkrsrc((jj & 1) ? Wk : Wq, D_ * HD_ * 2u), rv = mkrsrc(Wv, D_ * HD_ * 2u), ro = mkrsrc(Wo, HD_ * D_ * 2u);
    const unsigned oa = (unsigned)((t >> 3) * HD_ + h * DH_ + 64 * (jj >> 1) + (t & 7) * 8) * 2u, ov = (unsigned)((t >> 2) * HD_ + h * DH_ + 32 * vj + (t & 3) * 8) * 2u;
    auto round = [=, &v](int r) {
#pragma unroll
      for (int i = 0; i < 6; ++i)
        asm volatile("buffer_load_dwordx4 %0, %1, %2, %3 offen " RNT : "=v"(v[r][i])
                     : "v"(i < 4 ? oa : ov), "s"(i < 4 ? ra : rv), "s"((unsigned)(128 * r + (i < 4 ? 64 * (i & 1) + 512 * (i >> 1) : 512 * (i & 1))) * (HD_ * 2u)));
    };
#pragma unroll
    for (int r = 0; r < 2; ++r) { round(r); Q_BAR; }
    const int kr = (lo >> 3) + 2 * (lo & 4);
    const ldsp pw = lb + ((w >> 1) * 256 + 16 * (g >> 1) + 2 * (g & 1) + kr) * (SSTR * 2) + (16 * (w & 1) + 8 * (l & 1) + 2 * (l & 2)) * 2;
    const ldsp pw2 = lb + (512 + 16 * g + 2 * (l & 2) + kr) * (SSTR * 2) + (8 * w + 4 * (l & 1)) * 2;
    auto take1 = [&](int r) {
      if (r == 1 || r == 2) __syncthreads();
      asm volatile("s_waitcnt vmcnt(%6)" : "+v"(v[r][0]), "+v"(v[r][1]), "+v"(v[r][2]), "+v"(v[r][3]), "+v"(v[r][4]), "+v"(v[r][5])
                   : "i"(r < 3 ? 6 : (QK ? 1 : WOA + 1)));
      if (r == 1) asm volatile("" : "+v"(m0v[0]), "+v"(m0v[1]), "+v"(m0v[2]), "+v"(m0v[3]));
      if (r == 1 && !QK) asm volatile("buffer_load_dwordx4 %0, %1, %2, 0 offen sc1" : "=v"(rel) : "v"(rl_off(vid)), "s"(rw));
      if (r < 2) round(r + 2);
#pragma unroll
      for (int i = 0; i < 2; ++i)
        if (r == 1) *(lp4)(lb + L_M0 + (t + 512 * i) * 16) = u32x4{evens(m0v[2 * i][0], m0v[2 * i + 1][0]), odds(m0v[2 * i][0], m0v[2 * i + 1][0]), evens(m0v[2 * i][1], m0v[2 * i + 1][1]), odds(m0v[2 * i][1], m0v[2 * i + 1][1])};
#pragma unroll
      for (int i = r == 2 ? 0 : WOA; i < (!QK && r >= 2 ? (r == 2 ? WOA : 16) : 0); ++i)
        asm volatile("s_mov_b32 m0, %3\n s_nop 3\n buffer_load_dwordx4 %0, %1, %2 offen " ONT " lds" :: "v"((unsigned)((l >> 3) * (D_ * 2) + (l & 7) * 16)), "s"(ro),
                     "s"((unsigned)((h * DH_ + 8 * i) * D_ + 256 * jj + 64 * w) * 2u), "s"(lbu + L_WO + (unsigned)(w * 16 + i) * 1024u) : "memory");
#pragma unroll
      for (int i = 0; i < 6; ++i)
        *(u32x4*)&stg[(i < 4 ? ((t >> 2) & 1) * 256 + (t >> 3) + 64 * i : 512 + (t >> 2) + 128 * (i & 1)) * SSTR + (t & 3) * 8] =
            u32x4{evens(v[r][i][0], v[r][i][1]), evens(v[r][i][2], v[r][i][3]), odds(v[r][i][0], v[r][i][1]), odds(v[r][i][2], v[r][i][3])};
      __syncthreads();
#pragma unroll
      for (int u = 0; u < (QK ? 4 : 0); ++u) {
        const int kp = 2 * r + (u & 1) + 8 * (u >> 1);
#pragma unroll
        for (int q = 0; q < 4; ++q) {
          const u32x2 x = ldtr(pw + (64 * u + 32 * (q >> 1) + 4 * (q & 1)) * (SSTR * 2));
          w1[kp][4 * (q >> 1) + (q & 1)] = x[0]; w1[kp][4 * (q >> 1) + 2 + (q & 1)] = x[1];
        }
        const u32x2 x = ldtr(pw2 + 64 * u * (SSTR * 2)), y = ldtr(pw2 + (64 * u + 2) * (SSTR * 2));
        w2[kp] = u32x4{x[0], x[1], y[0], y[1]};
      }
    };
#pragma unroll
    for (int r = 0; r < 3; ++r) take1(r);
  Far fm;
  if constexpr (!QK) {
    asm volatile("" : "+v"(rel));
    fm = accept(rw, tag, rel);
    *(lp4)(lb + L_FM) = __builtin_bit_cast(u32x4, fm);
  }
  BAR();
  if constexpr (QK) {
    const u32x4 f = *(lp4)(lb + L_FM);
    fm = __builtin_bit_cast(Far, u32x4{RFL(f[0]), RFL(f[1]), RFL(f[2]), RFL(f[3])});
    st_sc1_64(rw, t == 0 ? W_HI + (unsigned)vid * 32u : ~0u, ~tag.t);
  }
  if (t < 20) *(lp1)(lb + L_T1 + t * 4) = 0u;

  const int idx = (l & 2) ? 0xeeee : 0x4444;
  unsigned pa = fm.blk<0>(256u) ^ 8192u, pb = fm.blk<0>(0u) ^ 8192u, pc = fm.blk<0>(128u) ^ 8192u;
  const unsigned ots = pb;
  const unsigned l16 = (unsigned)l * 16u;
  const unsigned o2 = (CH_R2 + (unsigned)h) << 14, o3 = ((CH_R3 + (unsigned)(jj * 16 + (h >> 1))) << 14) + (unsigned)(h & 1) * 4096u;
#define SKIP 1
  const int ow = (jj & 1) * 2 + (jj >> 1);
  const int OWNQ = SKIP ? L_QK + ow * 1024 : L_OWN;
  unsigned rc = 2u;
#define ZR2(a, b) "v_mov_b64 v[" #a ":" #b "], 0\n"
  auto lz16 = [&]() { unsigned lz = l16; asm volatile("" : "+v"(lz)); return lz; };
  auto next_step = [&]() { const unsigned a = pa; pa = pb; pb = pc; pc = a; ++rc; };
#define NOP4 "s_nop 1\n s_nop 1\n s_nop 1\n s_nop 1\n"
#define EXACT(t, x) "s_mov_b32 %[n], 256\n L" #x "%=:\n s_memtime vcc\n s_sub_u32 %[n], %[n], 1\n s_waitcnt lgkmcnt(0)\n s_sub_u32 %[a2], vcc_lo, %[" #t "]\n" \
                    "s_andn2_b32 vcc_hi, %[a2], %[n]\n s_cmp_lt_i32 vcc_hi, 0\n s_cbranch_scc1 L" #x "%=\n" \
                    "s_min_u32 %[a2], %[a2], 128\n s_lshr_b32 %[a2], %[a2], 3\n s_lshl2_add_u32 %[a2], %[a2], 12\n s_getpc_b64 vcc\n s_add_u32 vcc_lo, vcc_lo, %[a2]\n" \
                    "s_addc_u32 vcc_hi, vcc_hi, 0\n s_setpc_b64 vcc\n" NOP4 NOP4 NOP4 NOP4

  if constexpr (QK) {
    int seen = 0, nf = (w & 1) ? 2 * NSEG_ : 0;
    auto cno = [&](unsigned slot) { return fm.blk<P_CNT>(0) + (slot + (slot & ~127u)) * 64u; };
    auto look = [&]() {
      unsigned x;
      asm volatile("s_nop 4\n buffer_load_dword %0, off, %1, %2 sc1\n s_waitcnt vmcnt(0)" : "=v"(x) : "s"(rw), "s"(cno(C_KV + 4 * h + (w & 2))) : "memory");
      seen = __builtin_amdgcn_readfirstlane(x);
    };
    auto put2 = [&](u32x2 v, unsigned so) { asm volatile("s_mov_b64 exec, 1\n s_nop 3\n buffer_store_dwordx2 %0, off, %1, %2 sc1\n s_mov_b64 exec, -1" :: "v"(v), "s"(rw), "s"(so) : "memory"); };
    auto fetch = [&](bool must) {
      for (int it = 0; must && it < WAIT_CAP && seen <= nf; ++it) {
        if (it) __builtin_amdgcn_s_sleep(4);
        look();
      }
      asm volatile("" ::: "memory");
      if (!must && seen <= nf) return;
      const unsigned kvs = fm.blk<P_K>((unsigned)(nf * H_ + h + (w >> 1) * 2048)) ^ KVX, lz = lz16(), kvo = lz ^ ((lz >> 1) & (unsigned)(w * 64));
#pragma unroll
      for (int i = 0; i < 2; ++i)
        asm volatile("s_mov_b32 m0, %3\n v_xor_b32 v251, %4, %0\n s_nop 2\n buffer_load_dwordx4 %0, %1, %2 offen sc1 lds\n buffer_load_dwordx4 v251, %1, %2 offen offset:1024 sc1 lds\n"
                     "buffer_load_dwordx4 %0, %1, %2 offen offset:2048 sc1 lds\n buffer_load_dwordx4 v251, %1, %2 offen offset:3072 sc1 lds"
                     :: "v"(kvo), "s"(rw), "s"(kvs + (unsigned)i * 4096u), "s"(lbu + L_KV + (unsigned)((nf & 1) * 4 + w + i) * 4096u), "s"(w * 32) : "memory", "v251");
      nf = nf == NSEG_ - 2 ? 2 * NSEG_ : nf + 1;
    };
    auto landed = [&]() { drain_vm(); *(lp1)(lb + L_KF + w * 4) = (unsigned)nf; };
    const bool mst = vid == 0;
    constexpr unsigned XI = X1I | X2I << 10 | X3I << 20, X0 = XI + (X0E | X0E / 2 << 10 | X0E << 20);
    unsigned xs = X0, xa = X0, xn = X0;
    unsigned tx = 0u;
    int bump = 0;
    auto tune = [&](int s) {
      const unsigned lz = lz16();
      const u32x4 v = *(lp4)(lb + L_RP + (lz & 511u) * (STS / 16u));
      const float f = 1024.f * __builtin_amdgcn_rcpf((float)(v[3] & 0xffffu));
      int en = *(lp1)(lb + L_EN + (lz >> 2));
      unsigned y = xn;
      if (s == TSE) y = XI;
      unsigned m[3];
#pragma unroll
      for (int i = 0; i < 3; ++i) {
        m[i] = (unsigned)((float)(v[i] & 0xffffu) * (i == 1 ? 1024.f / CK2 : f));
        if ((v[i] >> 16) != (unsigned)(s - 1) || m[i] > ((y >> (10 * i)) & 1023u) + RLIM) m[i] = 0u;
#pragma unroll
        for (int k = 0; k < 4; ++k) asm("s_nop 1\n v_max_u32_dpp %0, %0, %0 row_ror:%1 row_mask:0xf bank_mask:0xf" : "+v"(m[i]) : "i"(8 >> k));
        m[i] = max((unsigned)__builtin_amdgcn_readlane(m[i], 0), (unsigned)__builtin_amdgcn_readlane(m[i], 16));
      }
      const unsigned sh = lz == 0u ? 0u : (lz == 16u ? 10u : 20u);
      const int x = (int)(y >> sh) & 1023, mg = lz == 0u ? M1 : (lz == 16u ? M2 : M3);
      const int a = min((int)(lz == 0u ? m[0] : (lz == 16u ? m[1] : m[2])), x - mg + RUPL) * 4, d = a - en;
      if (s == 0) en = -1;
      if (s >= TS0 && a != 0) en = en < 0 ? a : en + (d > 0 ? d >> RUP : d >> RDN);
      if (bump) en = max(en, (x - mg) * 4) + (bump > 1 ? BUMP2 : BUMP1) * (lz == 16u ? 2 : 4);
      *(lp1)(lb + L_EN + (lz >> 2)) = en;
      const int c = en < 0 ? x : min(max(max(((en + 2) >> 2) + mg, x - RDNL), 128), 1000);
      y = (unsigned)__builtin_amdgcn_readlane(c, 0) | (unsigned)__builtin_amdgcn_readlane(c, 1) << 10 | (unsigned)__builtin_amdgcn_readlane(c, 2) << 20;
      return y;
    };
    auto time0 = [&]() {
#pragma nounroll
      for (int it = 0; it < 64; ++it) {
        dma1(l16, rw, ots + (CH_RD << 14), lbu + L_MEM);
        drain_vm();
        const unsigned lz = lz16();
        u32x4 ra = *(lp4)(lb + L_MEM + lz);
        asm volatile("" : "+{v[220:223]}"(ra));
        const unsigned t0 = now10();
        unsigned m = min(min((t0 - ra[0]) << 1, (t0 - ra[1]) << 1), min((t0 - ra[2]) << 1, (t0 - ra[3]) << 1));
#pragma unroll
        for (int o = 32; o >= 1; o >>= 1) m = min(m, (unsigned)__builtin_amdgcn_ds_bpermute((int)(lz >> 2) ^ o * 4, (int)m));
        if (__all(max4(0u, ra) != ~0u)) return scalar((t0 - (m >> 1) + TA0) << 3);
      }
      return now10() << 3;
    };
    unsigned ce = RFL(clk32()), ve = now10() << 3, cpt = 0u;
#define RTS(i) "s_nop 1\n s_memrealtime s[" #i ":" #i "+1]\n"
#define RTA(i) "s_add_u32 s84, s84, s" #i "\n"
#define ANCH1 "s_memtime s[82:83]\n" RTS(84) RTS(86) RTS(88) RTS(90) RTS(92) RTS(94) RTS(96) RTS(98)
#define ANCHOR ANCH1 ANCH2
#define ANCH2 "s_waitcnt lgkmcnt(0)\n" RTA(86) RTA(88) RTA(90) RTA(92) RTA(94) RTA(96) RTA(98)
#define ANCLB "s82", "s83", "s84", "s85", "s86", "s87", "s88", "s89", "s90", "s91", "s92", "s93", "s94", "s95", "s96", "s97", "s98", "s99"
    unsigned c3 = 0u, c1 = 0u, c2 = 0u, t3 = 0u, t2 = 0u, slc = 0u;
    const unsigned psel = (l & 8) ? 0x03020706u : 0x05040100u;
    const unsigned oq = o2 + (unsigned)((jj & 1) * 2048 + (4 * (jj >> 1) + w) * 256 + l * 4);
    const unsigned ou = (l & 4) ? 1u << 31 : o2 + (unsigned)(4096 + (4 * vj + w) * 128 + (l >> 3) * 16 + (l & 3) * 4);
    unsigned pm = lbu + L_M0 + (unsigned)(((g >> 1) * 32 + (lo >> 2) * 8 + (lo & 1) * 4 + (g & 1) * 2 + ((lo >> 1) & 1)) * 16);
    int e0 = 2;
    int tries = 1, ab = 0;
    unsigned cv = 0u;
    unsigned kv = 0u, km = 0u, ca = 0u, rp = 0u;
    auto rows = [&](int s) {
      const unsigned sm = (unsigned)(h & 3) * 2048u, sb = (unsigned)(s * 8 + (h >> 2));
      kv = (jj & 1) ? fm.blk<P_KM>(sb) + sm + (unsigned)(jj >> 1) * 1024u : 0u;
      km = fm.blk<P_VM>(sb) + sm + (unsigned)vj * 512u;
      ca = cno(C_MEM + s * 32 - 32 + h);
    };
    if (w == 3) rows(0);
    u32x2 tv = {0u, ~0u};
    SCHED; take1(3);
#pragma nounroll
    for (int s = 0;;) {
#pragma nounroll
      for (;;) {
        unsigned wq, wu, ws_, n, a2, ex;
        unsigned long long bad, tj, tq;
        s = RFL(s); rc = RFL(rc); e0 = RFL(e0); tries = RFL(tries); kv = RFL(kv); seen = RFL(seen);
#define P1R(v0, v1, o) "ds_read_b128 v[" #v0 ":" #v1 "], %[pm] offset:" #o "\n"
#define P1M(n, v0, v1, k) "s_waitcnt lgkmcnt(" #n ")\n v_smfmac_f32_16x16x64_bf16 v[252:255], v[" #v0 ":" #v1 "], %[w" #k "], %[ix]\n"
#define P1V(v0, v1, k) "v_mfma_f32_16x16x32_bf16 v[216:219], v[" #v0 ":" #v1 "], %[u" #k "], v[216:219]\n"
#define P1W(n, v0, v1, k) "s_waitcnt lgkmcnt(" #n ")\n" P1V(v0, v1, k)
#define GA4(s) "buffer_load_dwordx4 v249, %[r], %[" #s "] offen sc1 lds\n buffer_load_dwordx4 v249, %[r], %[" #s "] offen offset:1024 sc1 lds\n" \
               "buffer_load_dwordx4 v249, %[r], %[" #s "] offen offset:2048 sc1 lds\n buffer_load_dwordx4 v249, %[r], %[" #s "] offen offset:3072 sc1 lds\n"
#define KV4(s) "buffer_load_dwordx4 v250, %[r], %[" #s "] offen sc1 lds\n buffer_load_dwordx4 v251, %[r], %[" #s "] offen offset:1024 sc1 lds\n" \
               "buffer_load_dwordx4 v250, %[r], %[" #s "] offen offset:2048 sc1 lds\n buffer_load_dwordx4 v251, %[r], %[" #s "] offen offset:3072 sc1 lds\n"
#define GAN(o) "s_add_u32 m0, m0, 4096\n s_add_u32 %[a2], %[pa], " #o "\n" GA4(a2)
#define RZ4(v, o) "buffer_store_dwordx4 v[216:219], v" #v ", %[r], %[pc] offen offset:" #o " sc1\n"
#define MG2(a, b) "v_and_b32 v" #a ", s90, v" #a "\n v_and_b32 v" #b ", s90, v" #b "\n v_pk_max_u16 v251, v251, v" #a "\n v_pk_max_u16 v251, v251, v" #b "\n"
#define MISS "s_mov_b32 s90, 0x7fff7fff\n s_mov_b32 s91, 0x7f817f81\n v_mov_b32 v251, s91\n s_add_u32 %[tr], %[tr], 1\n s_waitcnt lgkmcnt(0)\n" \
             MG2(220, 221) MG2(222, 223) MG2(224, 225) MG2(226, 227) MG2(228, 229) MG2(230, 231) MG2(232, 233) MG2(234, 235) \
             "v_cmp_ne_u32 vcc, s91, v251\n s_cmp_gt_u32 %[tr], %[cap]\n s_cselect_b32 %[ab], 1, %[ab]\n s_cmp_lg_u32 %[ab], 0\n s_cselect_b64 vcc, 0, vcc\n"
#define JM(n, b, c) "s_waitcnt vmcnt(" #n ")\n v_mfma_f32_16x16x32_bf16 v[252:255], v[240:243], v[" #b ":" #b "+3], " c "\n"
        asm volatile(
            "s_cmp_eq_u32 %[e0], 2\n s_cbranch_scc1 Lb%=\n s_cmp_eq_u32 %[e0], 3\n s_cbranch_scc1 Lp%=\n s_cmp_eq_u32 %[e0], 6\n s_cbranch_scc1 Lq%=\n La%=:\n s_setprio 3\n"
            "ds_read_b128 v[244:247], %[vo] offset:%[tj2]\n ds_read_b64 v[248:249], %[vo] offset:%[tj3]\n s_mov_b32 m0, %[lm]\n s_cmp_eq_u32 %[e0], 1\n s_cbranch_scc1 Lg%=\n"
            "ds_read_b128 v[236:239], %[vo] offset:%[tj0]\n ds_read_b128 v[240:243], %[vo] offset:%[tj1]\n v_mov_b64 v[216:217], -1\n v_mov_b64 v[218:219], -1\n"
            EXACT(c3, c)
            "buffer_load_dwordx4 v[220:223], v236, %[r], %[pa] offen sc1\n buffer_load_dwordx4 v[224:227], v237, %[r], %[pa] offen sc1\n"
            "buffer_load_dwordx4 v[228:231], v238, %[r], %[pa] offen sc1\n buffer_load_dwordx4 v[232:235], v239, %[r], %[pa] offen sc1\n"
            ANCH1 RZ4(246, 0) RZ4(246, 1024) RZ4(246, 2048) RZ4(246, 3072) RZ4(247, 0) RZ4(248, 0)
            "buffer_store_dword v216, v244, %[r], %[pb] offen sc1\n s_lshl_b32 %[a2], %[st], 7\n s_add_u32 %[a2], %[a2], %[ots]\n buffer_store_dword v216, v245, %[r], %[pb] offen sc1\n"
            "buffer_load_dwordx2 %[tv], off, %[r], %[a2] sc1\n"
            ANCH2
            "s_and_b32 %[n], %[xs], 1023\n s_bfe_u32 %[a2], %[xs], 0xa000a\n s_add_u32 %[n], %[t3], %[n]\n s_add_u32 %[t2], %[n], %[a2]\n s_sub_u32 %[n], %[n], s84\n s_mul_i32 %[n], %[n], %[cpt]\n"
            "s_mul_i32 %[a2], %[a2], %[ck]\n s_ashr_i32 %[n], %[n], 10\n s_lshr_b32 %[a2], %[a2], 10\n s_add_u32 %[c1], s82, %[n]\n s_add_u32 %[c2], %[c1], %[a2]\n"
            "s_mul_i32 %[slc], %[cpt], %[sl]\n s_lshr_b32 %[slc], %[slc], 10\n"
            "v_mov_b32 v236, %[c2]\n v_mov_b32 v237, %[slc]\n v_mov_b32 v251, %[lm]\n"
            "s_mov_b64 exec, 1\n ds_write_b64 v251, v[236:237] offset:%[t1]\n s_mov_b64 exec, -1\n ds_read_b32 v238, v251 offset:%[lv]+4\n"
            JM(12, 220, "0") JM(11, 224, "v[252:255]") JM(10, 228, "v[252:255]") JM(9, 232, "v[252:255]")
            "s_memtime %[tj]\n s_mov_b32 %[ex], 1\n s_nop 5\n"
            "v_add_f32_dpp v252, v252, v252 row_ror:8 row_mask:0xf bank_mask:0xf\n v_add_f32_dpp v253, v253, v253 row_ror:8 row_mask:0xf bank_mask:0xf\n"
            "v_cvt_pk_bf16_f32 %[w], v252, v253\n"
            "buffer_store_dword %[w], v244, %[r], %[pa] offen sc1\n buffer_store_dword %[w], v245, %[r], %[pa] offen sc1\n"
            "v_cmp_u_f32 vcc, v252, v253\n s_cbranch_vccnz Lu%=\n s_waitcnt lgkmcnt(0)\n v_cmp_eq_u32 vcc, %[rc], v238\n s_cbranch_vccnz Lv%=\n"
            "Lg%=:\n" EXACT(c1, d)
            GA4(pa) GAN(4096) GAN(16384) GAN(20480)
            "Lb%=:\n" ZR2(252, 253) ZR2(254, 255) "s_setprio 2\n s_mov_b32 m0, %[owq]\n"
            "s_waitcnt vmcnt(12) lgkmcnt(0)\n s_barrier\n"
            P1R(220, 223, 0) P1R(224, 227, 1024) P1R(228, 231, 2048) P1R(232, 235, 3072)
            "s_waitcnt vmcnt(8)\n s_barrier\n"
            P1R(236, 239, 4096) P1R(240, 243, 5120) P1M(5, 220, 223, 0) P1R(244, 247, 6144) P1M(5, 224, 227, 1) P1R(248, 251, 7168) P1M(5, 228, 231, 2) P1M(4, 232, 235, 3)
            "s_waitcnt vmcnt(4)\n s_barrier\n"
            P1R(220, 223, 8192) P1R(224, 227, 9216) P1M(5, 236, 239, 4) P1R(228, 231, 10240) P1M(5, 240, 243, 5) P1R(232, 235, 11264) P1M(5, 244, 247, 6) P1M(4, 248, 251, 7)
            "s_waitcnt vmcnt(0)\n s_barrier\n"
            P1R(236, 239, 12288) P1R(240, 243, 13312) P1R(244, 247, 14336) P1R(248, 251, 15360)
            P1M(7, 220, 223, 8) P1M(6, 224, 227, 9) P1M(5, 228, 231, 10) P1M(4, 232, 235, 11) P1M(3, 236, 239, 12) P1M(2, 240, 243, 13) P1M(1, 244, 247, 14) P1M(0, 248, 251, 15)
            "s_memtime %[tq]\n v_mfma_f32_16x16x32_bf16 v[216:219], v[220:223], %[u8], 0\n s_nop 5\n"
            "v_pk_add_f32 v[252:253], v[252:253], v[254:255]\n v_cvt_pk_bf16_f32 v254, v252, v253\n" P1V(224, 227, 9) "v_cmp_u_f32 %[bad], v252, v253\n"
            "v_mov_b32_dpp v255, v254 row_ror:8 row_mask:0xf bank_mask:0xf\n v_perm_b32 %[wq], v255, v254, %[pse]\n"
            "buffer_store_dword %[wq], %[oq], %[r], %[pb] offen sc1\n"
            P1V(228, 231, 10) "s_waitcnt lgkmcnt(0)\n" P1R(220, 223, 0) P1R(224, 227, 1024) P1V(232, 235, 11) P1R(228, 231, 2048)
            "v_mov_b32 v255, 0\n s_cmp_lg_u32 %[cv], 0\n" P1V(236, 239, 12) P1R(232, 235, 3072) "s_cselect_b64 exec, 1, 0\n buffer_load_dword v255, off, %[r], %[cv] sc1\n s_mov_b64 exec, -1\n" P1V(240, 243, 13) P1R(236, 239, 4096)
            P1V(244, 247, 14) P1R(240, 243, 5120) P1V(248, 251, 15) P1R(244, 247, 6144)
            P1W(6, 220, 223, 0) P1R(248, 251, 7168) P1W(6, 224, 227, 1) "ds_write_addtid_b32 %[wq]\n s_mov_b32 m0, %[own]\n"
            P1W(6, 228, 231, 2) P1W(5, 232, 235, 3) P1W(4, 236, 239, 4) P1W(3, 240, 243, 5) P1W(2, 244, 247, 6) P1W(1, 248, 251, 7)
            "s_mov_b32 %[ex], 2\n s_nop 6\n"
            "v_add_f32_dpp v216, v218, v216 row_ror:8 row_mask:0xf bank_mask:0x3\n v_add_f32_dpp v216, v217, v219 row_ror:8 row_mask:0xf bank_mask:0xc\n"
            "v_cmp_u_f32 vcc, v216, v216\n s_nop 0\n v_mov_b32_dpp v217, v216 row_shl:4 row_mask:0xf bank_mask:0xf bound_ctrl:0\n"
            "v_cvt_pk_bf16_f32 %[wu], v216, v217\n"
            "buffer_store_dword %[wu], %[ou], %[r], %[pb] offen sc1\n"
            "ds_write_addtid_b32 %[wu] offset:1024\n s_or_b64 %[bad], %[bad], vcc\n s_setprio 0\n s_cmp_eq_u32 %[st], 0\n s_cbranch_scc1 Lw%=\n s_cmp_lg_u64 %[bad], 0\n s_cbranch_scc1 Lw%=\n"
            "s_mov_b32 %[ex], 0\n s_cmp_eq_u32 %[kx], 192\n s_cbranch_scc1 Lq%=\n s_cmp_eq_u32 %[e0], 2\n s_cbranch_scc1 Lf%=\n s_waitcnt lgkmcnt(0)\n s_barrier\n"
            "Lp%=:\n s_mov_b32 %[ex], 0\n s_cmp_eq_u32 %[st], 63\n s_cbranch_scc1 Lx%=\n" LWJ "s_sleep %[da]\n Ll%=:\n" ANCHOR "s_cmp_gt_u32 %[e0], 3\n s_cbranch_scc1 Lh%=\n"
            "v_readfirstlane_b32 s87, v21\n v_readfirstlane_b32 s86, v20\n s_cmp_eq_u32 s87, -1\n s_cselect_b32 s87, %[xs], s87\n s_cselect_b32 s89, 1, 0\n"
            "s_lshr_b32 s88, s87, 20\n s_add_u32 s88, s88, %[t2]\n s_cmp_lg_u32 s89, 0\n s_cselect_b32 s86, s88, s86\n s_sub_u32 s89, s84, s88\n s_add_u32 s89, s89, %[lf]\n s_max_i32 s89, s89, %[sh]\n"
            "s_add_u32 s89, s89, s88\n s_sub_u32 s89, s89, s86\n s_cmp_gt_i32 s89, 0\n s_cbranch_scc1 Lk%=\n"
            "Lj%=:\n s_sub_u32 s88, s86, s84\n s_sub_u32 s90, s84, %[ve]\n s_sub_u32 s91, s82, %[ce]\n s_mul_i32 s92, s90, %[cpt]\n s_lshr_b32 s92, s92, 10\n s_sub_u32 s91, s91, s92\n"
            "s_flbit_i32_b32 s92, s90\n s_sub_u32 s92, 22, s92\n s_ashr_i32 s91, s91, s92\n s_cmp_lt_u32 s92, 10\n s_cselect_b32 s91, s91, 0\n s_add_u32 %[cpt], %[cpt], s91\n"
            "s_and_b32 s90, %[xs], 1023\n s_mul_i32 s90, s90, %[cpt]\n s_lshr_b32 s90, s90, 10\n s_add_u32 s90, s90, s80\n"
            "s_sub_u32 s90, s90, %[c1]\n s_sub_u32 s91, s78, %[c1]\n s_cmp_eq_u32 %[tr], 0\n s_cselect_b32 s90, s90, 0\n s_cselect_b32 s91, s91, 0\n"
            "s_mul_i32 s88, s88, %[cpt]\n s_ashr_i32 s88, s88, 10\n s_add_u32 %[c3], s82, s88\n s_mov_b32 %[tr], 0\n s_mov_b32 %[e0], 0\n"
            "v_mov_b32 v252, s90\n v_mov_b32 v253, s91\n v_mov_b32 v254, %[cpt]\n v_mov_b32 v251, %[lm]\n s_mov_b32 %[t3], s86\n s_mov_b32 %[xs], s87\n s_mov_b32 %[ce], s82\n s_mov_b32 %[ve], s84\n"
            "s_lshr_b32 s88, s87, 20\n s_mul_i32 s88, s88, %[cpt]\n s_lshr_b32 s88, s88, 10\n s_sub_u32 s88, %[c3], s88\n s_add_u32 %[st], %[st], 1\n"
            "v_mov_b32 v220, s86\n v_mov_b32 v221, %[st]\n v_mov_b32 v222, s88\n s_mov_b64 exec, 1\n ds_write_b96 v251, v[220:222] offset:%[t1]+64\n ds_max_u32 v251, v252 offset:%[la]\n ds_max_u32 v251, v253 offset:%[la]+4\n ds_write_b32 v251, v254 offset:%[la]+12\n s_mov_b64 exec, -1\n"
            "s_mov_b32 s90, %[pa]\n s_mov_b32 %[pa], %[pb]\n s_mov_b32 %[pb], %[pc]\n s_mov_b32 %[pc], s90\n s_add_u32 %[rc], %[rc], 1\n s_branch La%=\n"
            "Lk%=:\n s_add_u32 s86, s86, %[sl]\n s_sub_u32 s89, s89, %[sl]\n s_cmp_gt_i32 s89, 0\n s_cbranch_scc1 Lk%=\n s_branch Lj%=\n"
            "Lh%=:\n s_bfe_u32 s88, %[xs], 0xa000a\n s_sub_u32 s89, %[t2], s88\n s_cmp_eq_u32 %[e0], 4\n s_cselect_b32 s90, %[t3], s89\n s_sub_u32 s90, s84, s90\n s_add_u32 s90, s90, %[ll]\n"
            "Lm%=:\n s_add_u32 %[t3], %[t3], %[sl]\n s_add_u32 %[t2], %[t2], %[sl]\n s_add_u32 s89, s89, %[sl]\n s_sub_u32 s90, s90, %[sl]\n s_cmp_gt_i32 s90, 0\n s_cbranch_scc1 Lm%=\n"
            "s_sub_u32 s90, %[t3], s84\n s_sub_u32 s89, s89, s84\n s_mul_i32 s90, s90, %[cpt]\n s_mul_i32 s89, s89, %[cpt]\n s_mul_i32 s88, s88, %[ck]\n s_ashr_i32 s90, s90, 10\n"
            "s_ashr_i32 s89, s89, 10\n s_lshr_b32 s88, s88, 10\n s_add_u32 %[c3], s82, s90\n s_add_u32 %[c1], s82, s89\n s_add_u32 %[c2], %[c1], s88\n"
            "v_mov_b32 v252, %[c2]\n v_mov_b32 v251, %[lm]\n s_sub_u32 %[e0], %[e0], 4\n ds_write_b32 v251, v252 offset:%[t1]\n s_branch La%=\n"
            "Lv%=:\n s_cmp_lg_u32 %[tr], 0\n s_cbranch_scc1 Lg%=\n s_add_u32 %[tr], %[tr], 1\n s_mov_b32 %[e0], 5\n s_branch Ll%=\n"
            "Lw%=:\n s_waitcnt vmcnt(1)\n v_add_u32 v250, %[qo], %[vo]\n ds_read_b128 v[220:223], v250\n ds_read_b128 v[224:227], v250 offset:1024\n ds_read_b128 v[228:231], v250 offset:2048\n"
            "ds_read_b128 v[232:235], v250 offset:3072\n"
            "Lu%=:\n" FLG MISS "s_cmp_eq_u32 %[ex], 2\n s_cbranch_scc1 Lt%=\n s_mov_b32 %[e0], 4\n s_cbranch_vccnz Ll%=\n s_waitcnt vmcnt(0)\n s_branch Lx%=\n"
            "Lt%=:\n s_cmp_eq_u32 %[st], 0\n s_cbranch_scc1 Lx%=\n v_mov_b32 v251, %[lm]\n v_mov_b32 v250, %[rc]\n s_cbranch_vccz Ly%=\n ds_write_b32 v251, v250 offset:%[lv]\n"
            "Ly%=:\n s_waitcnt lgkmcnt(0)\n s_barrier\n ds_read_b32 v250, v251 offset:%[lv]\n s_waitcnt lgkmcnt(0)\n v_cmp_eq_u32 vcc, %[rc], v250\n s_cbranch_vccz Lx%=\n"
            "s_add_u32 %[rc], %[rc], 1\n s_cmp_eq_u32 %[e0], 2\n s_cbranch_scc1 Lb%=\n s_mov_b32 %[e0], 5\n s_branch Ll%=\n"
            "Lq%=:\n v_and_b32 v250, 0x100, %[vo]\n v_and_b32 v251, 32, %[vo]\n v_lshl_add_u32 v250, v251, 2, v250\n v_lshrrev_b32 v251, 1, %[vo]\n v_and_b32 v251, 104, v251\n"
            "v_lshl_add_u32 v250, v250, 1, v251\n v_and_b32 v251, 112, %[vo]\n v_and_b32 v249, 0x370, %[vo]\n v_bfe_u32 v248, %[vo], 7, 1\n v_add3_u32 v251, v251, v249, %[owq]\n"
            "v_lshl_add_u32 v251, v248, 4, v251\n v_subrev_u32 v251, 768, v251\n v_mov_b32 v249, 1\n s_cmp_eq_u32 %[e0], 6\n s_mov_b32 %[e0], 2\n s_waitcnt lgkmcnt(0)\n s_cbranch_scc1 Lr%=\n s_barrier\n"
            "Lr%=:\n ds_read_b64_tr_b16 v[224:225], v250 offset:%[lo]\n ds_read_b64_tr_b16 v[226:227], v250 offset:%[lo]+128\n ds_read_b128 v[220:223], v251\n"
            "s_mov_b32 %[ex], 0\n s_cmp_eq_u32 %[st], 0\n s_cselect_b64 exec, 0, 1\n s_cbranch_scc1 Ls%=\n buffer_atomic_add v249, off, %[r], %[ca]\n Ls%=:\n s_mov_b64 exec, -1\n s_waitcnt lgkmcnt(1)\n"
            "v_min_u32 v224, -2, v224\n v_min_u32 v225, -2, v225\n v_min_u32 v226, -2, v226\n v_min_u32 v227, -2, v227\n s_mov_b32 exec_hi, 0\n"
            "buffer_store_dwordx4 v[224:227], %[vo], %[r], %[km] offen sc1\n s_mov_b32 exec_hi, -1\n s_cmp_eq_u32 %[kv], 0\n s_cbranch_scc1 Lx%=\n s_waitcnt lgkmcnt(0)\n"
            "v_min_u32 v220, -2, v220\n v_min_u32 v221, -2, v221\n v_min_u32 v222, -2, v222\n v_min_u32 v223, -2, v223\n"
            "buffer_store_dwordx4 v[220:223], %[vo], %[r], %[kv] offen sc1\n s_branch Lx%=\n"
            "Lf%=:\n v_lshrrev_b32 v248, 2, %[vo]\n v_mov_b32 v250, 0\n s_cmp_lg_u32 %[rp], 0\n s_cselect_b64 exec, 7, 0\n ds_wrxchg_rtn_b32 v249, v248, v250 offset:%[la]\n s_mov_b64 exec, 8\n"
            "ds_read_b32 v249, v248 offset:%[la]\n s_mov_b64 exec, -1\n s_mov_b32 m0, %[km]\n v_lshrrev_b32 v250, 1, %[vo]\n v_and_b32 v250, %[kx], v250\n v_xor_b32 v250, %[vo], v250\n"
            "s_lshr_b32 %[n], %[kx], 1\n v_xor_b32 v251, %[n], v250\n s_lshl_b32 %[n], %[st], 16\n s_add_u32 %[a2], %[kv], 4096\n s_waitcnt lgkmcnt(0)\n v_min_u32 v249, 0xffff, v249\n"
            "v_or_b32 v249, %[n], v249\n s_waitcnt vmcnt(1)\n v_readfirstlane_b32 %[n], v255\n s_max_i32 %[se], %[se], %[n]\n s_cmp_gt_i32 %[se], %[ca]\n s_cselect_b32 %[kv], %[kv], 0\n"
            "s_cmp_eq_u32 %[rp], 0\n s_barrier\n s_cbranch_scc1 Ln%=\n s_mov_b64 exec, 15\n buffer_store_dword v249, v248, %[r], %[rp] offen sc1\n s_mov_b64 exec, -1\n"
            "Ln%=:\n s_cmp_eq_u32 %[kv], 0\n s_cbranch_scc1 Lx%=\n" KV4(kv) "s_add_u32 m0, m0, 4096\n s_nop 0\n" KV4(a2)
            "Lx%=:\n s_setprio 0\n s_waitcnt lgkmcnt(0)\n"
            : [wq] "=&v"(wq), [wu] "=&v"(wu), [bad] "=&s"(bad), [ex] "=&s"(ex), [n] "=&s"(n), [a2] "=&s"(a2), [tj] "=&{s[80:81]}"(tj), [tq] "=&{s[78:79]}"(tq), [tv] "+{v[20:21]}"(tv),
              [pa] "+s"(pa), [pb] "+s"(pb), [pc] "+s"(pc), [st] "+s"(s), [rc] "+s"(rc), [c3] "+s"(c3), [c1] "+s"(c1), [c2] "+s"(c2), [t3] "+s"(t3), [t2] "+s"(t2), [slc] "+s"(slc),
              [xs] "+s"(xs), [ce] "+s"(ce), [ve] "+s"(ve), [cpt] "+s"(cpt), [e0] "+s"(e0), [tr] "+s"(tries), [ab] "+s"(ab), [kv] "+s"(kv), [se] "+s"(seen),
              [w] "=&{v250}"(ws_)
            : [pm] "v"(pm), [oq] "v"(oq), [ou] "v"(ou), [pse] "v"(psel), [ix] "v"(idx), [vo] "v"(l16), [r] "s"(rw),
              [ots] "s"(ots), [cv] "s"(RFL(cv)), [km] "s"(RFL(km)), [ca] "s"(RFL(ca)), [rp] "s"(RFL(rp)), [kx] "s"(w * 64), [qo] "s"(lbu + L_MEM + (unsigned)w * 4096u), [lm] "s"(lbu + L_MEM), [own] "s"(lbu + L_OWN + (unsigned)w * 256u), [owq] "s"(lbu + OWNQ + (unsigned)w * 256u),
              [tj0] "i"(L_J), [tj1] "i"(L_J + 1024), [tj2] "i"(L_J + 2048), [tj3] "i"(L_J + 3072), [t1] "i"(L_T1), [la] "i"(L_A), [sl] "i"(SLOT), [sh] "i"(-(int)SLOT / 2), [lf] "i"(LEADF), [ll] "i"(LEADL), [da] "i"(DLYA), [ck] "i"(CK2), [lv] "i"(L_V), [cap] "i"(TRY_CAP), [lo] "i"(L_OWN + 1024),
              [w0] "{v[24:31]}"(w1[0]), [w1] "{v[32:39]}"(w1[1]), [w2] "{v[40:47]}"(w1[2]), [w3] "{v[48:55]}"(w1[3]), [w4] "{v[56:63]}"(w1[4]), [w5] "{v[64:71]}"(w1[5]), [w6] "{v[72:79]}"(w1[6]), [w7] "{v[80:87]}"(w1[7]),
              [w8] "{v[88:95]}"(w1[8]), [w9] "{v[96:103]}"(w1[9]), [w10] "{v[104:111]}"(w1[10]), [w11] "{v[112:119]}"(w1[11]), [w12] "{v[120:127]}"(w1[12]), [w13] "{v[128:135]}"(w1[13]), [w14] "{v[136:143]}"(w1[14]), [w15] "{v[144:151]}"(w1[15]),
              [u0] "{v[152:155]}"(w2[0]), [u1] "{v[156:159]}"(w2[1]), [u2] "{v[160:163]}"(w2[2]), [u3] "{v[164:167]}"(w2[3]), [u4] "{v[168:171]}"(w2[4]), [u5] "{v[172:175]}"(w2[5]), [u6] "{v[176:179]}"(w2[6]), [u7] "{v[180:183]}"(w2[7]),
              [u8] "{v[184:187]}"(w2[8]), [u9] "{v[188:191]}"(w2[9]), [u10] "{v[192:195]}"(w2[10]), [u11] "{v[196:199]}"(w2[11]), [u12] "{v[200:203]}"(w2[12]), [u13] "{v[204:207]}"(w2[13]), [u14] "{v[208:211]}"(w2[14]), [u15] "{v[212:215]}"(w2[15])
            : "memory", "vcc", "scc", "v216", "v217", "v218", "v219", VR10(22), VR10(23), VR10(24), "v251", "v252", "v253", "v254", "v255", ANCLB);
        pa = RFL(pa); pb = RFL(pb); pc = RFL(pc); s = RFL(s); rc = RFL(rc); c3 = RFL(c3); c1 = RFL(c1); c2 = RFL(c2); t3 = RFL(t3); t2 = RFL(t2); slc = RFL(slc);
        xs = RFL(xs); ce = RFL(ce); ve = RFL(ve); cpt = RFL(cpt); e0 = RFL(e0); tries = RFL(tries); ab = RFL(ab); kv = RFL(kv); seen = RFL(seen);
        if (__builtin_expect(ex == 0u, 1)) break;
        const unsigned lz = lz16();
        if (ex == 1u) {
          const u32x2 rz = *(lp2)(lb + L_J + 2048 + lz);
          ws_ = settle(ws_);
          __builtin_amdgcn_raw_buffer_store_b32(ws_, rw, rz[0], pa, 16); __builtin_amdgcn_raw_buffer_store_b32(ws_, rw, rz[1], pa, 16);
          e0 = 1;
          continue;
        }
        if (bad != 0ull) {
          __builtin_amdgcn_raw_buffer_store_b32(settle(wq), rw, oq, pb, 16);
          __builtin_amdgcn_raw_buffer_store_b32(settle(wu), rw, ou, pb, 16);
        }
        if (s == 0) {
          if (w == JW) {
            const unsigned ln = lz >> 4, one = (ln & 2u) ? 0u : ((ln & 1u) ? 0x3f800000u : 0x3f80u);
            const unsigned z = ((CH_R1 + (unsigned)(jj >> 1)) << 14) + (ln & 8u) * 4096u + (unsigned)((jj & 1) * 32 + (h & 24) + (h & 1) * 4 + ((h >> 1) & 3)) * 128u + (ln >> 4) * 32u + (ln & 7u) * 4u;
            const unsigned zq = o2 + (unsigned)((jj & 1) * 2048 + (jj >> 1) * 1024) + lz, zv = lz < 512u ? o2 + (unsigned)(4096 + 512 * vj) + lz : 1u << 31;
            *(lp4)(lb + L_J + lz) = ((CH_R3 + (unsigned)jj * 16u) << 14) + (ln >> 4) * 16384u + (ln & 8u) * 512u + (unsigned)h * 128u + (lz & 127u) + u32x4{0u, 65536u, 131072u, 196608u};
            *(lp4)(lb + L_J + 1024 + lz) = u32x4{(ln & 12u) == 0u ? one : 0u, (ln & 12u) == 4u ? one : 0u, (ln & 12u) == 8u ? one : 0u, (ln & 12u) == 12u ? one : 0u};
            *(lp4)(lb + L_J + 2048 + lz) = u32x4{z, z + 65536u, o3 + lz, zq};
            *(lp2)(lb + L_J + 3072 + lz) = u32x2{zv, ((CH_R1 + (unsigned)(vid >> 5) * 2u) << 14) + lz};
            const u32x4 on = {~0u, ~0u, ~0u, ~0u};
#pragma unroll
            for (int i = 0; i < 6; ++i) __builtin_amdgcn_raw_buffer_store_b128(on, rw, i < 4 ? o3 + lz + i * 1024u : (i == 4 ? zq : zv), pc, 16);
          }
          if (!(w & 1)) fetch(true);
          st_sc1_32(rw, (w & 1) || lz ? ~0u : ots + (CH_RD << 14) + (unsigned)(vid * 8 + w * 2), now10() & 0x7fffffffu);
          landed();
          BAR();
        }
        if (w == 3) { e0 = 6; continue; }
        kv = 0u;
        break;
      }
      int sz = s;
      asm volatile("" : "+s"(sz));
      if (UNLIKELY(sz == NSEG_ - 1)) break;
      if (w == JW) {
        if (sz == 0) {
          t2 = time0() - (X0 >> 20);
          cpt = scalar((unsigned)((float)vec(clk32() - ce) * 1024.f * __builtin_amdgcn_rcpf((float)vec((now10() << 3) - ve))));
        }
        e0 = 3;
      } else {
        if (w == 3) {
          if (mst) {
            const unsigned cl = RFL(*(lp1)(lb + L_T1));
            unsigned y = tune(sz);
            u32x2 ta;
#pragma nounroll
            for (int it = 0; it < 512; ++it) {
              asm volatile("" ::: "memory");
              ta = *(lp2)(lb + L_TA);
              if (RFL(*(lp1)(lb + L_T1)) != cl) break;
              __builtin_amdgcn_s_sleep(1);
            }
            dma1(l16, rw, W_ST + (unsigned)sz * (32u * STS), lbu + L_RP);
            if (RFL(ta[1]) != (unsigned)sz + 1u) y = xn;
            else put2(u32x2{ta[0] + (xa & 1023u) + ((xa >> 10) & 1023u) + (xn & 1023u) + ((xn >> 10) & 1023u) + (xn >> 20) + (y >> 20), y}, ots + (unsigned)(sz + 2) * 128u);
            bump = sz && RFL(ta[0]) != tx ? bump + 1 : 0;
            tx = RFL(ta[0]) + (xa & 1023u) + ((xa >> 10) & 1023u) + (xn >> 20);
            xa = xn; xn = y;
          }
          rows(sz + 1);
        } else {
          *(lp1)(lb + L_KF + w * 4) = (unsigned)nf;
          const bool late = nf <= sz + 1;
          if (kv) nf = nf == NSEG_ - 2 ? 2 * NSEG_ : nf + 1;
          if (UNLIKELY(nf <= sz + 1)) fetch(true);
          if (UNLIKELY(late)) landed();
          if (UNLIKELY(nf <= sz + 2) && (sz == 0 || seen > nf + KVL)) fetch(false);
          LWD;
          kv = nf <= sz + 3 ? fm.blk<P_K>((unsigned)(nf * H_ + h + (w >> 1) * 2048)) ^ KVX : 0u;
          km = lbu + L_KV + (unsigned)((nf & 1) * 4 + w) * 4096u;
          ca = nf;
          cv = seen <= nf + 1 + KVL && nf < NSEG_ ? cno(C_KV + 4 * h + (w & 2)) : 0u;
          rp = w == 2 && ((sz + 1) & 3) == jj ? W_ST + (unsigned)(sz * 32 + 32 + h) * STS : 0u;
        }

        next_step(); ++s; tries = 0;
      }
      pm = lbu + ((pm - lbu) & 1023u);
      asm volatile("" : "+v"(pm));
    }
    drain_vm();
  } else {
    const unsigned long long him = __builtin_amdgcn_ballot_w64((l & 2) != 0);
    const u32x4 bsum = u32x4{0x3f803f80u, g < 2 ? 0x3f803f80u : 0u, 0x3f803f80u, 0u};
    const int idv = (l & 4) ? 0xeeee : 0x4444;
    const unsigned ovm = o2 + (unsigned)(4096 + (2 * g + ((lo >> 2) & 1)) * 128 + (4 * (lo >> 3) + (lo & 3)) * 16);
    const unsigned pq = lbu + (unsigned)(L_QK + (g >> 1) * 256 + (g & 1) * 16 + (2 * (lo >> 2) + (l & 1)) * 32), pk = lbu + (unsigned)(L_QK + 2048 + (g >> 1) * 256 + (g & 1) * 16 + (lo & 7) * 32);
    const unsigned oq = o2 + (unsigned)w * 1024u + l16, op = o3 + (unsigned)w * 1024u + l16;
    const int er = 16 * (g >> 1) + 4 * (g & 1) + (lo >> 2);
    const unsigned pvx = lbu + (unsigned)(L_KV + 8192 + (g & 1) * 1024 + (l & 2) * 128 + ((8 * (g >> 1) + (lo >> 2)) ^ (4 * (g & 1) + 4 * (l & 2))) * 16 + (l & 1) * 8);
    SCHED; take1(3);
    asm volatile("s_waitcnt lgkmcnt(0)\n s_barrier\n s_barrier\n s_barrier\n s_barrier\n s_waitcnt vmcnt(0)" ::: "memory");
    u32x8 w3[8], ub[4];
#pragma unroll
    for (int i = 0; i < 32; ++i) {
      const u32x2 x = ldtr(lb + L_WO + w * 16384 + (er + 32 * ((i >> 1) & 3) + 8 * (i & 1)) * 128 + (16 * (l & 2) + 8 * (i >> 3) + 4 * (l & 1)) * 2);
      w3[i >> 2][4 * ((i >> 1) & 1) + (i & 1)] = x[0]; w3[i >> 2][4 * ((i >> 1) & 1) + 2 + (i & 1)] = x[1];
    }
#pragma unroll
    for (int i = 0; i < 4; ++i) ub[i] = u32x8{0u, 0u, 0u, 0u, 0u, 0u, 0u, 0u};
    u32x4 kf[8];
#pragma unroll
    for (int i = 0; i < 8; ++i) kf[i] = u32x4{0u, 0u, 0u, 0u};
    unsigned ux = 0u;
    unsigned c2 = clk32(), slc = 2048u;
#pragma nounroll
    for (int s = 0; s < NSEG_; ++s) {
#pragma nounroll
      for (int it = ux ? SPIN_CAP * 32 : 0; it < SPIN_CAP * 32; ++it) {
        asm volatile("" ::: "memory");
        const u32x4 f = *(lp4)(lb + L_KF);
        if (__builtin_amdgcn_readfirstlane(min(min(f[0], f[1]), min(f[2], f[3]))) > (unsigned)s) break;
        __builtin_amdgcn_s_sleep(1);
      }
      asm volatile("" ::: "memory");
#define UXR(v0, v1, v2, v3, o) "ds_read_b64_tr_b16 v[" #v0 ":" #v1 "], %[p0] offset:" #o "\n ds_read_b64_tr_b16 v[" #v2 ":" #v3 "], %[p1] offset:" #o "\n"
#define UXS(a, b) "v_swap_b32 v" #a ", v" #b "\n"
#define UXK(k, o) "ds_read_b128 %[k" #k "], %[pf] offset:" #o "\n"
#define UXM(d0, d1, a0, a1, b0, b1, c) "v_mfma_f32_16x16x32_bf16 v[" #d0 ":" #d1 "], v[" #a0 ":" #a1 "], v[" #b0 ":" #b1 "], " c "\n"
#define UXN(d0, d1, a0, a1, b0, b1) UXM(d0, d1, a0, a1, b0, b1, "v[" #d0 ":" #d1 "]")
#define UX1(a0, a1, b0, b1, c0, c1, d0, d1, e0, e1, x) UXN(220, 223, a0, a1, b0, b1) x UXN(224, 227, a0, a1, c0, c1) UXN(228, 231, a0, a1, d0, d1) UXN(232, 235, a0, a1, e0, e1)
#define UX2(a0, a1, b0, b1, c0, c1, d0, d1, e0, e1, x, y) UXN(236, 239, a0, a1, b0, b1) x UXN(240, 243, a0, a1, c0, c1) y UXN(244, 247, a0, a1, d0, d1) UXN(248, 251, a0, a1, e0, e1)
#define ATW(n) "s_waitcnt lgkmcnt(" #n ")\n"
#define CVT(d, a, b) "v_cvt_pk_bf16_f32 v" #d ", v" #a ", v" #b "\n"
#define W3P "{v[64:71]}"(w3[0]), "{v[72:79]}"(w3[1]), "{v[80:87]}"(w3[2]), "{v[88:95]}"(w3[3]), "{v[96:103]}"(w3[4]), "{v[104:111]}"(w3[5]), "{v[112:119]}"(w3[6]), "{v[120:127]}"(w3[7])
#define UBP "+{v[128:135]}"(ub[0]), "+{v[136:143]}"(ub[1]), "+{v[144:151]}"(ub[2]), "+{v[152:159]}"(ub[3])
#define UXA UXR(164, 165, 166, 167, 0) UXR(168, 169, 170, 171, 2048) UXR(172, 173, 174, 175, 4096) UXR(176, 177, 178, 179, 6144) \
            UXR(180, 181, 182, 183, 512) UXR(184, 185, 186, 187, 2560) UXR(188, 189, 190, 191, 4608)
#define UXB ATW(12) UXS(165, 166) UXR(192, 193, 194, 195, 6656) \
            UXM(220, 223, 164, 167, 64, 67, "0") ATW(12) UXS(169, 170) UXM(224, 227, 164, 167, 80, 83, "0") UXM(228, 231, 164, 167, 96, 99, "0") UXM(232, 235, 164, 167, 112, 115, "0") \
            UX1(168, 171, 68, 71, 84, 87, 100, 103, 116, 119, ATW(10) UXS(173, 174)) UX1(172, 175, 72, 75, 88, 91, 104, 107, 120, 123, ATW(8) UXS(177, 178)) \
            UX1(176, 179, 76, 79, 92, 95, 108, 111, 124, 127, ATW(0) UXS(181, 182)) \
            UXM(236, 239, 180, 183, 64, 67, "0") UXS(185, 186) UXK(0, 0) UXM(240, 243, 180, 183, 80, 83, "0") UXS(189, 190) UXK(1, 1024) \
            UXM(244, 247, 180, 183, 96, 99, "0") UXS(193, 194) UXK(2, 2048) UXM(248, 251, 180, 183, 112, 115, "0") UXK(3, 3072) UXK(4, 4096) UXK(5, 5120) UXK(6, 6144) UXK(7, 7168) \
            UX2(184, 187, 68, 71, 84, 87, 100, 103, 116, 119, CVT(128, 220, 221) CVT(129, 222, 223), CVT(136, 224, 225) CVT(137, 226, 227)) \
            UX2(188, 191, 72, 75, 88, 91, 104, 107, 120, 123, CVT(144, 228, 229) CVT(145, 230, 231), CVT(152, 232, 233) CVT(153, 234, 235)) \
            UX2(192, 195, 76, 79, 92, 95, 108, 111, 124, 127, "", "") ATW(0) "s_nop 3\n" \
            CVT(132, 236, 237) CVT(133, 238, 239) CVT(140, 240, 241) CVT(141, 242, 243) CVT(148, 244, 245) CVT(149, 246, 247) CVT(156, 248, 249) CVT(157, 250, 251)
#define KFP [k0] "+v"(kf[0]), [k1] "+v"(kf[1]), [k2] "+v"(kf[2]), [k3] "+v"(kf[3]), [k4] "+v"(kf[4]), [k5] "+v"(kf[5]), [k6] "+v"(kf[6]), [k7] "+v"(kf[7])
#define UXIN(s) [p0] "v"(pvx + (unsigned)((s) & 1) * 16384u), [p1] "v"((pvx ^ 64u) + (unsigned)((s) & 1) * 16384u), [pf] "v"(lbu + L_KV + (unsigned)((s) & 1) * 16384u + l16), W3P
      asm volatile("s_cmp_lg_u32 %[ux], 0\n s_cbranch_scc1 Lz%=\n" UXA UXB "Lz%=:\n" : UBP, KFP : UXIN(s), [ux] "s"(__builtin_amdgcn_readfirstlane(ux))
                   : "memory", "scc", VR8(16, 4, 5, 6, 7, 8, 9, 9, 9), VR10(17), VR10(18), VR8(19, 0, 1, 2, 3, 4, 5, 5, 5), VR10(22), VR10(23), VR10(24), "v250", "v251");
      unsigned a3 = 0u;
      int mode = s ? 0 : 2;
#pragma nounroll
      for (int pass = 0;;) {
        unsigned n, a2, f1, tg;
        unsigned long long bad3, bb, tq;
        u32x4 vm0, vm1, x3;
#define ATR(v0, v1, p, o) "ds_read_b128 v[" #v0 ":" #v1 "], %[" #p "] offset:" #o "\n"
#define ATX(d0, d1, k, b0, b1, c) "v_mfma_f32_16x16x32_bf16 v[" #d0 ":" #d1 "], %[k" #k "], v[" #b0 ":" #b1 "], " c "\n"
#define ATM(a0, a1, b0, b1, c) "v_mfma_f32_16x16x32_bf16 v[204:207], v[" #a0 ":" #a1 "], v[" #b0 ":" #b1 "], " c "\n"
#define UMM(d0, d1, a, b0, b1) "v_smfmac_f32_16x16x64_bf16 v[" #d0 ":" #d1 "], %[vm" #a "], v[" #b0 ":" #b1 "], %[iv]\n"
#define PUM(d0, d1, b0, b1) "v_smfmac_f32_16x16x64_bf16 v[" #d0 ":" #d1 "], v[212:215], v[" #b0 ":" #b1 "], %[ix]\n"
#define SEL(d, a) "v_cndmask_b32 v" #d ", v" #d ", v" #a ", %[hi]\n"
#define FMA(a) "v_fma_f32 v" #a ", v" #a ", %[sc1], -v208\n"
#define EXP(a) "v_exp_f32 v" #a ", v" #a "\n"
#define SWP(a, b) "v_permlane32_swap_b32 v" #a ", v" #b "\n"
#define PKA(a, b, c, d) "v_pk_add_f32 v[" #a ":" #b "], v[" #a ":" #b "], v[" #c ":" #d "]\n"
#define PKM2(a, b, c, d) "v_pk_mul_f32 v[" #a ":" #b "], v[" #a ":" #b "], v[" #c ":" #d "]\n"
        asm volatile(
            "s_mov_b32 m0, %[m]\n v_mov_b32 v210, %[pt]\n s_mov_b32 %[f1], 0\n s_mov_b32 %[ux], 0\n s_mov_b64 %[bad], 0\n s_mov_b32 %[tg], %[c2]\n"
            ZR2(220, 221) ZR2(222, 223) ZR2(224, 225) ZR2(226, 227) ZR2(228, 229) ZR2(230, 231) ZR2(232, 233) ZR2(234, 235)
            ZR2(236, 237) ZR2(238, 239) ZR2(240, 241) ZR2(242, 243) ZR2(244, 245) ZR2(246, 247) ZR2(248, 249) ZR2(250, 251)
            "s_cmp_eq_u32 %[md], 3\n s_cbranch_scc1 Lq%=\n s_cmp_eq_u32 %[md], 2\n s_cbranch_scc1 Lo%=\n"
            "s_waitcnt lgkmcnt(0)\n s_barrier\n ds_read_b64 v[208:209], v210\n s_barrier\n s_waitcnt lgkmcnt(0)\n"
            "v_readfirstlane_b32 %[c2], v208\n v_readfirstlane_b32 %[slc], v209\n s_barrier\n s_cmp_eq_u32 %[st], 63\n s_barrier\n s_memtime vcc\n s_cbranch_scc1 Ly%=\n"
            "s_waitcnt lgkmcnt(0)\n s_sub_u32 %[tg], vcc_lo, %[c2]\n s_add_u32 %[tg], %[tg], %[dm]\n s_max_i32 %[tg], %[tg], 0\n s_add_u32 %[tg], %[tg], %[c2]\n"
            EXACT(tg, c) "Lo%=:\n s_cmp_eq_u32 %[sk], 0\n"
            "Lm%=:\n s_cbranch_scc1 Lq%=\n buffer_load_dwordx4 %[oq], %[r], %[pb] offen sc1 lds\n Lq%=:\n s_add_u32 %[tg], %[tg], %[dv]\n"
            EXACT(tg, d)
            "buffer_load_dwordx4 %[vm0], %[ovm], %[r], %[pb] offen sc1\n buffer_load_dwordx4 %[vm1], %[ovm], %[r], %[pb] offen offset:1024 sc1\n"
            "s_setprio 1\n s_cmp_lg_u32 %[md], 3\n s_cbranch_scc1 Lb%=\n s_waitcnt vmcnt(2)\n s_branch Ls%=\n"
            "Ly%=:\n s_barrier\n ds_read_b32 v211, v210 offset:%[ov]\n s_waitcnt lgkmcnt(0)\n v_cmp_eq_u32 vcc, %[rc], v211\n s_cbranch_vccz Lz%=\n"
            "Lf%=:\n s_mov_b32 %[f1], 1\n s_setprio 0\n s_waitcnt vmcnt(0)\n s_nop 7\n s_branch Lz%=\n"
            "Ln%=:\n s_cmp_gt_u32 %[md], 1\n s_cbranch_scc1 Lz%=\n s_add_u32 %[a2], %[rc], 1\n v_mov_b32 v211, %[a2]\n ds_write_b32 v210, v211 offset:%[ov]+4\n s_branch Lj%=\n"
            "Lb%=:\n s_waitcnt vmcnt(2)\n" "s_barrier\n Ls%=:\n"
            ATR(164, 167, pq, 0) ATR(168, 171, pq, 512) ATR(172, 175, pq, 1024) ATR(176, 179, pq, 1536)
            ATR(180, 183, pk, 0) ATR(184, 187, pk, 512) ATR(188, 191, pk, 1024) ATR(192, 195, pk, 1536) "ds_read_b32 v211, v210 offset:%[ov]\n"
            ATW(8) ATX(196, 199, 0, 164, 167, "0") ATX(200, 203, 4, 164, 167, "0")
            ATW(7) ATX(196, 199, 1, 168, 171, "v[196:199]") ATX(200, 203, 5, 168, 171, "v[200:203]")
            ATW(6) ATX(196, 199, 2, 172, 175, "v[196:199]") ATX(200, 203, 6, 172, 175, "v[200:203]")
            ATW(5) ATX(196, 199, 3, 176, 179, "v[196:199]") ATX(200, 203, 7, 176, 179, "v[200:203]")
            ATW(0) "v_cmp_eq_u32 vcc, %[rc], v211\n s_nop 0\n" ATM(180, 183, 164, 167, "0")
            "s_cbranch_vccnz Lf%=\n s_nop 1\n" ATM(184, 187, 168, 171, "v[204:207]")
            SEL(196, 200) SEL(197, 201)
            ATM(188, 191, 172, 175, "v[204:207]")
            SEL(198, 202) SEL(199, 203) "v_max3_f32 v208, v196, v197, v198\n"
            ATM(192, 195, 176, 179, "v[204:207]")
            "v_max_f32 v208, v208, v199\n s_nop 1\n v_max_f32_dpp v208, v208, v208 quad_perm:[2,3,0,1] row_mask:0xf bank_mask:0xf\n"
            "v_mov_b32 v209, v208\n s_nop 1\n" SWP(208, 209)
            "v_max3_f32 v210, v204, v205, v206\n v_max3_f32 v208, v208, v209, v207\n"
            "v_max_f32 v208, v208, v210\n v_mul_f32 v209, 0x3e0293ee, v208\n v_mul_f32 v208, 0x3e0293ee, v208\n"
            SEL(204, 206) SEL(205, 207) "v_permlane16_swap_b32 v208, v209\n s_waitcnt vmcnt(0)\n" UMM(220, 223, 0, 64, 71) "v_max_f32 v208, v208, v209\n"
            FMA(196) UMM(220, 223, 1, 72, 79) FMA(197) EXP(196) UMM(224, 227, 0, 80, 87) FMA(198) EXP(197) UMM(224, 227, 1, 88, 95) FMA(199) EXP(198)
            UMM(228, 231, 0, 96, 103) FMA(204) EXP(199) UMM(228, 231, 1, 104, 111) FMA(205) EXP(204) UMM(232, 235, 0, 112, 119) EXP(205) UMM(232, 235, 1, 120, 127)
            CVT(212, 196, 197) CVT(214, 198, 199) CVT(213, 204, 205) CVT(215, 204, 205) CVT(130, 220, 221) CVT(131, 222, 223)
            "v_mfma_f32_16x16x32_bf16 v[216:219], v[212:215], %[bs], 0\n"
            CVT(138, 224, 225) CVT(139, 226, 227) PUM(236, 239, 128, 135) CVT(146, 228, 229) CVT(147, 230, 231) PUM(240, 243, 136, 143)
            CVT(154, 232, 233) CVT(155, 234, 235) PUM(244, 247, 144, 151) PKA(216, 217, 218, 219) "v_rcp_f32 v252, v216\n" PUM(248, 251, 152, 159)
            "v_rcp_f32 v253, v217\n" PKA(236, 237, 238, 239) PKM2(236, 237, 252, 253) PKA(240, 241, 242, 243) PKM2(240, 241, 252, 253) CVT(160, 236, 240)
            PKA(244, 245, 246, 247) CVT(162, 237, 241) PKA(248, 249, 250, 251) PKM2(244, 245, 252, 253) PKM2(248, 249, 252, 253) CVT(161, 244, 248) CVT(163, 245, 249)
            "buffer_store_dwordx4 v[160:163], %[op], %[r], %[pb] offen sc1\n"
            "s_memtime %[tq]\n s_setprio 0\n"
            PKA(236, 237, 240, 241) PKA(244, 245, 248, 249)
            "v_cmp_u_f32 %[bb], v236, v237\n v_cmp_u_f32 %[bad], v244, v245\n s_waitcnt lgkmcnt(0)\n s_or_b64 %[bad], %[bad], %[bb]\n"
            "v_mov_b32 v210, %[pt]\n s_cbranch_scc1 Ln%=\n ds_read_b128 v[196:199], v210 offset:%[ok]\n" UXA
            "s_waitcnt lgkmcnt(14)\n v_min_u32 v196, v196, v197\n v_min3_u32 v196, v196, v198, v199\n s_add_u32 %[a2], %[st], 1\n v_readfirstlane_b32 %[ux], v196\n"
            "s_cmp_gt_u32 %[ux], %[a2]\n s_cselect_b32 %[ux], 1, 0\n s_cbranch_scc0 Lj%=\n" UXB
            "Lj%=:\n s_waitcnt lgkmcnt(0)\n Lz%=:\n"
            : [n] "=&s"(n), [a2] "=&s"(a2), [f1] "=&s"(f1), [ux] "=&s"(ux), [tg] "=&s"(tg), [c2] "+s"(c2), [slc] "+s"(slc), [bad] "=&s"(bad3), [bb] "=&s"(bb), [tq] "=&s"(tq),
              [vm0] "=&v"(vm0), [vm1] "=&v"(vm1), "=&{v[160:163]}"(x3), UBP, KFP
            : [md] "s"(mode), [st] "s"(s), [m] "s"(lbu + L_QK + (unsigned)w * 1024u), [pt] "s"(lbu + L_T1), [rc] "s"(rc), [r] "s"(rw), [pb] "s"(pb),
              [oq] "v"(oq), [op] "v"(op), [ovm] "v"(ovm), [pq] "v"(pq + (pass ? (unsigned)(L_WO - L_QK + w * 4096) : 0u)), [pk] "v"(pk + (pass ? (unsigned)(L_WO - L_QK + w * 4096) : 0u)),
              [hi] "s"(him), [sc1] "s"(0x3e0293ee), [bs] "v"(bsum), [ix] "v"(idx), [iv] "v"(idv), [sk] "s"(SKIP ? w ^ ow : 1), [dm] "i"(DMIN), [dv] "i"(DLYV),
              [ok] "i"(L_KF - L_T1), [ov] "i"(L_V - L_T1), UXIN(s + 1)
            : "memory", "vcc", "scc", VR8(16, 4, 5, 6, 7, 8, 9, 9, 9), VR10(17), VR10(18), VR10(19), VR10(20), VR10(21), VR10(22), VR10(23), VR10(24),
              VR8(25, 0, 1, 2, 3, 4, 5, 5, 5));
        c2 = RFL(c2); slc = RFL(slc); ux = RFL(ux);
        if (UNLIKELY(RFL(f1))) { ++rc; mode = 1; continue; }

        if (mode == 0) a3 = (unsigned)tq;
        if (__builtin_expect(bad3 == 0ull, 1)) break;
        a3 = 0u;
        const unsigned lz = lz16(), own = (unsigned)(L_WO + w * 4096);
        if (pass) {
          unsigned mg = mag4(mag4(0u, vm0), vm1);
#pragma unroll
          for (int i = 0; i < 4; ++i) mg = mag4(mg, *(lp4)(lb + own + i * 1024 + lz));
          if (pass > TRY_CAP) abort_ = true;
          if (abort_ || !__any(missing(mg))) {
            __builtin_amdgcn_raw_buffer_store_b128(u32x4{settle(x3[0]), settle(x3[1]), settle(x3[2]), settle(x3[3])}, rw, op, pb, 16);
            break;
          }
        }
        ++pass; mode = 3;
        c2 += slots(c2 + E2, clk32() - TOL2, slc) * slc;
        for (int it = 0; it < 256; ++it) if ((int)(c2 + E2 - clk32()) <= 0) break;
#pragma unroll
        for (int i = 0; i < 4; ++i) dma1(l16, rw, pb + o2 + 1024u * i, lbu + own + 1024u * i);
      }
      const u32x4 ta = *(lp4)(lb + L_TA);
      a3 = a3 != 0u && ta[1] == (unsigned)s + 1u ? a3 - ta[2] : 0u;
      asm volatile("s_mov_b64 exec, 1\n ds_max_u32 %0, %1 offset:%2\n s_mov_b64 exec, -1" :: "v"(lbu), "v"(a3), "i"(L_A + 8) : "memory");
      next_step();
    }
  }
}


constexpr unsigned long long mkprog(int a, int b = 31, int c = 31, int d = 31, int e = 31, int f = 31, int g = 31, int h = 31, int i = 31, int j = 31, int k2 = 31) {
  const int v[12] = {a, b, c, d, e, f, g, h, i, j, k2, 31};
  unsigned long long p = 0;
  for (int k = 11; k >= 0; --k) p = p << 5 | (unsigned)v[k];
  return p;
}
#define PROG0 mkprog(1, 3)
#define PROG1 mkprog(2, 3)
#define PROG2 mkprog(4, 0, 3)
#define PROG3 mkprog(16, 18, 19, 20, 21, 24, 25, 22, 23, 26, 27)
#define PROG4 mkprog(16, 18, 19, 24, 20, 21, 25, 22, 23, 26, 27)
#define PROG5 mkprog(16, 18, 19, 20, 24, 21, 22, 25, 23, 26, 27)
#define PROG6 mkprog(16, 18, 19, 20, 21, 24, 22, 23, 25, 26, 27)
#define STAG 1
#define PAUSEM 2

#define FXOR 0x0FF0000000000008ull
#define VCL 300u
#define VW 4
#define VG 600
#define ROT 24
DEVI unsigned vpage(unsigned j, unsigned w) { return (4u * j + (w & 3u) + 65u * (w >> 2)) & 127u; }
DEVI int cast_vote(const int vid, char* ws, rsrc_t rw) {
  const unsigned l = threadIdx.x & 63u, w = __builtin_amdgcn_readfirstlane(threadIdx.x >> 6), pg = vpage((unsigned)vid - 224u, w);
  const unsigned la = (pg < NPG ? (pg << 21) + ((pg >> 2 & 7u) << 10) : ((pg & 3u) << 21) + ((pg - 104u) >> 2 << 10)) + (126u << 14) + (w >> 2) * 24576u;
  const unsigned oa = l < 8u ? la + l * 16u : 1u << 31, ob = oa ^ 8192u;
  unsigned long long c[10];
#define VT(x, y, o, a, b) "buffer_store_dwordx4 %[d], %[" #x "], %[r], 0 offen offset:" #o " sc1\n buffer_store_dwordx4 %[d], %[" #y "], %[r], 0 offen offset:" #o " sc1\n" \
                          "s_waitcnt vmcnt(1)\n s_memtime %[c" #a "]\n s_waitcnt vmcnt(0)\n s_memtime %[c" #b "]\n"
  asm volatile(VT(oa, ob, 0, 0, 1) VT(ob, oa, 128, 2, 3) VT(oa, ob, 512, 4, 5) VT(ob, oa, 640, 6, 7) VT(oa, ob, 768, 8, 9) "s_waitcnt lgkmcnt(0)"
               : [c0] "=&s"(c[0]), [c1] "=&s"(c[1]), [c2] "=&s"(c[2]), [c3] "=&s"(c[3]), [c4] "=&s"(c[4]), [c5] "=&s"(c[5]), [c6] "=&s"(c[6]), [c7] "=&s"(c[7]),
                 [c8] "=&s"(c[8]), [c9] "=&s"(c[9])
               : [oa] "v"(oa), [ob] "v"(ob), [d] "v"(u32x4{~0u, ~0u, ~0u, ~0u}), [r] "s"(rw) : "memory");
  int vote = 0;
#pragma unroll
  for (int r = 0; r < 5; ++r) {
    const int t2 = (int)min((unsigned)c[2 * r + 1] - (unsigned)c[2 * r], VCL);
    vote += (r & 1) ? t2 : -t2;
  }
  return ((pg ^ w >> 2) & 1u) ? -vote : vote;
}
DEVI void setup(char* lds, const Tag& tag, char* ws, rsrc_t rw) {
  typedef __attribute__((address_space(3))) int* lpi;
  const ldsp lb = (ldsp)lds;
  const int vid = tag.vid;
  const unsigned t = (unsigned)tid(), l = t & 63u, w = __builtin_amdgcn_readfirstlane(t >> 6), j = (unsigned)vid - 192u;
  if (j < 32u) {
    constexpr unsigned N0 = NCH * 1024u, N1 = N0 + 128u * STS, N2 = N1 + (C_SLOTS + 127u) / 128u * 1024u;
#pragma nounroll
    for (unsigned i = (31u - j) * 512u + w * 64u; i < N2; i += 16384u) {
      const unsigned v = i < N1 ? ~0u : 0u;
      st_sc1(rw, (i + l) * 16u + (i < N0 ? 0u : (i < N1 ? W_ST - N0 * 16u : ((unsigned)P_CNT << 21) - N1 * 16u)), u32x4{v, v, v, v});
    }
    if (j - 1u < 5u && w == 0u) {
      float sn, cs;
      sincospif((float)l / (float)(32u << j), &sn, &cs);
      st_sc1_64(rw, W_TW + (j - 1u) * 512u + l * 8u, u32x2{__float_as_uint(cs), __float_as_uint(-sn)});
    }
  }
  report(rw, tag);
  if (vid != MST) return;
  if (t < 192u) *(lpi)(lb + t * 4) = 0;
  u32x4 m;
  u32x2 a;
#pragma nounroll
  for (int it = 0; it < WAIT_CAP; ++it) {
    asm volatile("" ::: "memory");
    m = ld_sc1(rw, W_MSG + t * 16u);
    a = ld_sc1_64(rw, W_HI + (t >> 1) * 32u);
    asm volatile("" : "+v"(a));
    if (__all(m[0] == tag.t[0] && m[1] == tag.t[1])) break;
  }
  __syncthreads();
  const unsigned p = vpage(t >> 3, t & 7u);
  if (t >= 256u) __hip_atomic_fetch_add((lpi)(lb + (VW + (p < NPG ? p : p & 3u)) * 4), (int)m[2], __ATOMIC_RELAXED, __HIP_MEMORY_SCOPE_WORKGROUP);
  __syncthreads();
  int e[2] = {0, 0}, n = 0;
#pragma unroll
  for (int i = 0; i < 2; ++i) {
#pragma unroll
    for (int k = 0; k <= 2 * VW; ++k) e[i] += *(lpi)(lb + (i * 64 + k) * 4 + l * 4);
    n += __popcll(__builtin_amdgcn_ballot_w64(e[i] > 0)) - __popcll(__builtin_amdgcn_ballot_w64(e[i] < 0));
  }
  n = n > 0 ? -VG : VG;
  const unsigned long long m0 = __builtin_amdgcn_ballot_w64(e[0] > n) ^ FXOR, m1 = __builtin_amdgcn_ballot_w64(e[1] > n), mm = ((t & 1u) ? m1 : m0) ^ 0xAAAAAAAAAAAAAAAAull;
  st_sc1(rw, W_RL + (t >> 1) * 128u + (t & 1u) * 16u, u32x4{a[0], a[1], (unsigned)mm, (unsigned)(mm >> 32)});
}
#define NAW 4
#define WOP 0
#define TKA 8
#define PAUSE3 6
constexpr int NMX = 6, LMX = 6 * NOS / NMX, LEND = LMX + 2 * NSEG_ - 6 * NOS;
DEVI void helper(const int vid, char* lds, const u16* __restrict__ x, const u16* __restrict__ Wq, const u16* __restrict__ Wk,
                 const u16* __restrict__ Wv, const u16* __restrict__ Wo, const u16* __restrict__ wr, const u16* __restrict__ wi,
                 const u16* __restrict__ gw, char* ws, const Tag& tag, u16* __restrict__ out) {
  const int hw = vid - 128;
  const rsrc_t rw = mkrsrc(ws, W_END);
  const int q = hw & 3, grp = hw >> 5;
  Far fm = {0ull, 0ull};
  if (grp >= 2) setup(lds, tag, ws, rw);
  unsigned long long prog = grp == 0 ? PROG0 : (grp == 1 ? PROG1 : (grp == 2 ? PROG2 : PROG3));
  if (STAG && grp == 3) prog = (hw & 31) < 12 ? PROG4 : ((hw & 31) < 24 ? PROG5 : PROG6);
  if (grp == 3) { for (int i = 0; i < PAUSE3; ++i) __builtin_amdgcn_s_sleep(32); drain_vm(); }
#pragma nounroll
  for (;;) {
    __syncthreads();
    asm volatile("" : "+s"(fm.m0), "+s"(fm.m1));
    unsigned* cnt = (unsigned*)(ws + fm.blk<P_CNT>(0));
    const int op = (int)prog & 31;
    const int n = (hw >> 2) & 7;
    if (op == 31) break;
    prog >>= 5;
    if (op < 3) {
      const int v = __builtin_amdgcn_readfirstlane(tid() >> 8) + (op & 2);
      if (v == 3) fm = qkv_server<true, 1>(lds, 2, q * 8 + n, x, Wv, ws, rw, tag);
      else if (v == 2) fm = qkv_server<true, 0>(lds, 2, q * 8 + n, x, Wv, ws, rw, tag);
      else if (v == 1) fm = qkv_server<false, 1>(lds, op, q * 8 + n, x, op ? Wk : Wq, ws, rw, tag);
      else fm = qkv_server<false, 0>(lds, op, q * 8 + n, x, op ? Wk : Wq, ws, rw, tag, fm, WOP && op == 0 && __builtin_amdgcn_readfirstlane(tid() >> 6) == 0 ? Wo : nullptr);
      continue;
    }
    if (op == 3) {
      if (__builtin_amdgcn_readfirstlane(tid() >> 8)) qkv_server<false, 1, true>(lds, grp, q * 8 + n, x, Wo, ws, rw, tag, fm);
      else qkv_server<false, 0, true>(lds, grp, q * 8 + n, x, Wo, ws, rw, tag, fm);
      continue;
    }
    if (op < 16) { for (int i = 0; i < PAUSEM * (op - 2); ++i) __builtin_amdgcn_s_sleep(32); drain_vm(); continue; }
    if (op < 24) {
      const int tz = tid(), kind = (op >> 1) & 3, i = 2 * (hw & 31) + (op & 1);
      if (kind == 0) { fm = filter_gate(lds, hw & 31, wr, wi, x, gw, rw, tag); cnt = (unsigned*)(ws + fm.blk<P_CNT>(0)); }
      else if (kind == 1) fft_rows_fwd(lds, i, x, ws, rw, fm);
      else {
        if (tz < 2) cnt_wait(cnt, C_FFT + kind - 1 - tz, 64u);
        __syncthreads();
        if (kind == 2) fft_cols(lds, i, ws, rw, fm);
        else fft_rows_inv(lds, i, ws, rw, fm);
      }
      drain_vm();
      __syncthreads();
      if (tz == 0) cnt_add(cnt, C_FFT + kind, op == 16 ? 2u : 1u);
      continue;
    }
    const int tz = tid();
    {
      const int h = q * 8 + __builtin_amdgcn_readfirstlane(tz >> 6), l = tz & 63, a = op < 26 ? 16 * (op - 24) + 2 * n : (op == 26 ? 32 + 2 * (n - NAW) : 48 - 2 * NAW + n);
      if (op >= 26 && (op == 27) != (n < NAW)) continue;
      if (l < (op < 27 ? 5 : 3)) cnt_wait(cnt, l < 2 ? C_KV + 4 * h + 2 * l : (l == 2 ? C_Q + 2 * h : C_MEM + (a + l - 3) * 32 + h), l < 3 ? (op < 27 ? a + 2 : NSEG_) : 4u);
      asm volatile("" ::: "memory");
      if (op < 27) xattn_wave<true>(l, a, 1, a + 2, h, ws, rw, fm, cnt);
      else xattn_wave<false>(l, a, NAW, NSEG_, h, ws, rw, fm, cnt);
    }
  }
  unsigned* cnt = (unsigned*)(ws + fm.blk<P_CNT>(0));
  unsigned* tk = cix(cnt, C_TICKET + q * 8 + __builtin_amdgcn_readfirstlane(tid() >> 6));
  auto ticket = [&]() { unsigned v = 0u; if ((tid() & 63) == 0) v = __hip_atomic_fetch_add(tk, 1u, __ATOMIC_RELAXED, __HIP_MEMORY_SCOPE_AGENT); return v; };
  unsigned nx = ticket();
  if ((tid() & 63) == 0) cnt_wait(cnt, C_FFT + 3, 64u);
#pragma nounroll
  for (;;) {
    asm volatile("" : "+s"(fm.m0), "+s"(fm.m1) :: "memory");
    const int n = __builtin_amdgcn_readfirstlane(nx);
    if (n >= LEND) break;
    if (n < LMX - TKA) nx = ticket();
    if (n < LMX) mix<NMX>(NMX * n, 1, q, rw, fm, cnt, out);
    else mix<1>(6 * NOS + n - LMX, 1, q, rw, fm, cnt, out);
    if (n >= LMX - TKA) nx = ticket();
  }
}

__global__ __launch_bounds__(512, 1) void fused_kernel(char* ws, const u16* __restrict__ x, const u16* __restrict__ mem0,
                                                       const u16* __restrict__ Wq, const u16* __restrict__ Wk,
                                                       const u16* __restrict__ Wv, const u16* __restrict__ Wo, u16* __restrict__ out,
                                                       const u16* __restrict__ wr, const u16* __restrict__ wi,
                                                       const u16* __restrict__ gw) {
  __shared__ __attribute__((aligned(16))) char lds[LDS_BYTES];
  unsigned long long t0;
  asm volatile("Lfused_entry:\n s_getpc_b64 s[92:93]\n v_lshlrev_b32 v254, 4, %2\n s_lshr_b32 s94, %1, 3\n s_bitcmp1_b32 %1, 2\n s_cselect_b32 s95, %3, 0\n s_add_u32 s94, s94, s95\n s_and_b32 s94, s94, 31\n"
               "s_lshl_b32 s96, s94, 13\n s_add_u32 s95, s95, s94\n s_cmp_lt_u32 s95, %3 + 24\n s_cselect_b32 s94, Lfused_end-Lfused_entry, 0\n s_mov_b32 s95, 0x20000\n"
               "s_and_b32 s93, s93, 0xffff\n v_readfirstlane_b32 s97, v254\n s_add_u32 m0, s97, %4\n s_cmp_lt_u32 s94, 0x40000\n s_cselect_b32 s94, s94, 0\n"
               "buffer_load_dwordx4 v254, s[92:95], s96 offen lds\n s_memrealtime %0"
               : "=s"(t0) : "s"(blockIdx.x), "v"(threadIdx.x), "i"(ROT), "s"((unsigned)(size_t)(ldsp)lds + 147456u)
               : "memory", "scc", "s92", "s93", "s94", "s95", "s96", "s97", "v254");
  __builtin_amdgcn_sched_barrier(0);
  asm volatile("s_waitcnt lgkmcnt(0)" : "+s"(t0) : "s"(ws), "s"(x), "s"(mem0), "s"(Wq), "s"(Wk), "s"(Wv), "s"(Wo), "s"(out), "s"(wr), "s"(wi), "s"(gw));
  ws += (0u - (unsigned)(size_t)ws) & 0x1fffffu;
  const int xc = blockIdx.x & 7, tt = blockIdx.x >> 3, vid = xc < 4 ? xc * 32 + tt : 124 + ((tt + ROT) & 31) * 4 + xc;
  if (threadIdx.x == 0) {
    const u32x2 n = __builtin_bit_cast(u32x2, t0);
    *(lp2)((ldsp)lds + LDS_BYTES - 16) = n;
    st_sc1_64(mkrsrc(ws, W_END), W_HI + (unsigned)vid * 32u, n);
  }
  int vote = 0;
  if (vid >= 224) vote = cast_vote(vid, ws, mkrsrc(ws, W_END));
  BAR();
  const u32x2 tv = *(lp2)((ldsp)lds + LDS_BYTES - 16);
  const Tag tag = {u32x2{(unsigned)__builtin_amdgcn_readfirstlane(tv[0]), (unsigned)__builtin_amdgcn_readfirstlane(tv[1])}, vid, vote};
  if (vid < 128) {
    const int wv = __builtin_amdgcn_readfirstlane(threadIdx.x >> 6);
    if (wv < 4) recurrence<true>(vid, wv, lds, mem0, Wq, Wk, Wv, Wo, ws, tag);
    else { recurrence<false>(vid, wv, lds, mem0, Wq, Wk, Wv, Wo, ws, tag); __builtin_amdgcn_s_waitcnt(0x0F70); }
  }
  else { helper(vid, lds, x, Wq, Wk, Wv, Wo, wr, wi, gw, ws, tag, out); __builtin_amdgcn_s_waitcnt(0x0F70); }
}
__device__ __attribute__((used, noinline)) void code_end() { asm volatile("Lfused_end:"); }

extern "C" void kernel_launch(void* const* d_in, const int* in_sizes, int n_in, void* d_out, int out_size, void* d_ws,
                              size_t ws_size, hipStream_t stream) {
  char* ws = (char*)d_ws;
  fused_kernel<<<256, 512, 0, stream>>>(ws, (const u16*)d_in[0], (const u16*)d_in[1], (const u16*)d_in[2], (const u16*)d_in[3],
                                        (const u16*)d_in[4], (const u16*)d_in[5], (u16*)d_out, (const u16*)d_in[8],
                                        (const u16*)d_in[9], (const u16*)d_in[10]);
}
```
